```python
import torch

def _softmax(x, axis=-1):
    return torch.softmax(x, dim=axis)
def _gelu(x):
    return torch.nn.functional.gelu(x, approximate="tanh")
def _silu(x):
    return torch.nn.functional.silu(x)
def _sigmoid(x):
    return torch.sigmoid(x)
def _layernorm(x, axis=-1, eps=1e-5):
    m = x.mean(dim=axis, keepdim=True)
    v = x.var(dim=axis, keepdim=True, unbiased=False)
    return (x - m) / torch.sqrt(v + eps)
def _scatter_add(tgt, idx, src, axis=0):
    return tgt.clone().index_add_(axis, idx, src)
def _segment_sum(src, seg, n):
    out = torch.zeros((n,) + src.shape[1:], dtype=src.dtype)
    return out.index_add_(0, seg, src)
def _pad(x, pw):
    flat = tuple(v for pair in reversed(pw) for v in pair)
    return torch.nn.functional.pad(x, flat)

kernel_name = 'gated_mix__linear_attn_block__dense_activation_mish__prompt'

def setup_inputs():

    _w = (lambda a: (lambda t: t.to(torch.bfloat16) if t.is_floating_point() else t)(torch.as_tensor(a)))
    def _A_setup():
        B, S, D = 1, 2048, 512
        return {
            "x": _w((lambda *s: torch.randn(*s, dtype=torch.bfloat16) * 0.05)(B, S, D)),
            "wqkv": _w((lambda *s: torch.randn(*s, dtype=torch.bfloat16) * 0.8)(D, 3 * D)),
            "wo": _w((lambda *s: torch.randn(*s, dtype=torch.bfloat16) * 0.1)(D, D)),
        }
    _A_in = _A_setup()

    def _B_setup():
        B, S, D, F = 1, 2048, 512, 1792
        return {
            "x": _w((lambda *s: torch.randn(*s, dtype=torch.bfloat16) * 0.05)(B, S, D)),
            "w1": _w((lambda *s: torch.randn(*s, dtype=torch.bfloat16) * 12.8)(D, F) * (D ** -0.5)),
            "b1": _w((lambda *s: torch.randn(*s, dtype=torch.bfloat16) * 0.4)(F)),
            "w2": _w((lambda *s: torch.randn(*s, dtype=torch.bfloat16) * 0.05)(F, D) * (F ** -0.5)),
        }
    _B_in = _B_setup()

    return {
        "A_x": _A_in["x"],
        "A_wqkv": _A_in["wqkv"],
        "A_wo": _A_in["wo"],
        "B_x": _B_in["x"],
        "B_w1": _B_in["w1"],
        "B_b1": _B_in["b1"],
        "B_w2": _B_in["w2"],
        "gate_w": _w((lambda *s: torch.randn(*s, dtype=torch.bfloat16) * 0.05)(512) * 16.0),
    }

def reference(A_x, A_wqkv, A_wo, B_x, B_w1, B_b1, B_w2, gate_w):
    def _A_ref(x, wqkv, wo):
        B, S, D = x.shape
        H = 32; Dh = D // H
        qkv = (lambda x, *d: x.permute(*d))((x @ wqkv).reshape(B, S, 3, H, Dh), 2, 0, 3, 1, 4)
        q, k, v = qkv[0], qkv[1], qkv[2]
        phi = lambda t: torch.where(t > 0, t + 1, torch.exp(t)) + 1e-6
        pq, pk = phi(q), phi(k)
        kv = torch.einsum("bhsd,bhse->bhde", pk, v)
        z = torch.einsum("bhsd->bhd", pk)
        num = torch.einsum("bhqd,bhde->bhqe", pq, kv)
        den = torch.einsum("bhqd,bhd->bhq", pq, z)[..., None] + 1e-6
        o = num / den
        return (lambda x, *d: x.permute(*d))(o, 0, 2, 1, 3).reshape(B, S, D) @ wo

    def _B_ref(x, w1, b1, w2):
        h = torch.einsum("bsd,df->bsf", x, w1) + b1
        sp = torch.log1p(torch.exp(-torch.abs(h))) + torch.where(h > 0, h, torch.zeros_like(h))
        act = h * torch.tanh(sp)
        return torch.einsum("bsf,fd->bsd", act, w2)

    x = A_x
    g = _sigmoid((x * gate_w).sum(-1, keepdims=True))
    a = _A_ref(x, A_wqkv, A_wo)
    b = _B_ref(x, B_w1, B_b1, B_w2)
    return g * a + (1 - g) * b
```

```cpp
#include <hip/hip_runtime.h>
#include <hip/hip_bf16.h>
#include <stdint.h>

typedef unsigned short u16;
__device__ __forceinline__ float bf2f(u16 b) { return __uint_as_float(((unsigned)b) << 16); }
__device__ __forceinline__ u16 f2bf(float f) { unsigned u = __float_as_uint(f); u += 0x7FFFu + ((u >> 16) & 1u); return (u16)(u >> 16); }

constexpr int S = 2048, D = 512, F = 1792, H = 32, DH = 16;

__global__ void k_gemm(const u16* A, int lda, const u16* W, int N, int K, float* C) {
  __shared__ float As[16][17];
  __shared__ float Ws[16][17];
  int tx = threadIdx.x, ty = threadIdx.y;
  int row = blockIdx.y * 16 + ty, col = blockIdx.x * 16 + tx;
  float acc = 0.f;
  for (int k0 = 0; k0 < K; k0 += 16) {
    As[ty][tx] = bf2f(A[row * lda + k0 + tx]);
    Ws[ty][tx] = bf2f(W[(k0 + ty) * N + col]);
    __syncthreads();
    for (int k = 0; k < 16; ++k) acc += As[ty][k] * Ws[k][tx];
    __syncthreads();
  }
  C[row * N + col] = acc;
}

__device__ __forceinline__ float phi(float t) { return (t > 0.f ? t + 1.f : __expf(t)) + 1e-6f; }

__global__ void k_kv(const float* qkv, float* kv, float* z) {
  int h = blockIdx.x, t = threadIdx.x;
  int d = t >> 4, e = t & 15;
  float acc = 0.f, zz = 0.f;
  for (int s = 0; s < S; ++s) {
    float pk = phi(qkv[s * 1536 + 512 + h * 16 + d]);
    float v = qkv[s * 1536 + 1024 + h * 16 + e];
    acc += pk * v; zz += pk;
  }
  kv[h * 256 + t] = acc;
  if (e == 0) z[h * 16 + d] = zz;
}

__global__ void k_o(const float* qkv, const float* kv, const float* z, u16* o) {
  int idx = blockIdx.x * blockDim.x + threadIdx.x;
  int s = idx >> 9, c = idx & 511, h = c >> 4, e = c & 15;
  float num = 0.f, den = 0.f;
  for (int d = 0; d < 16; ++d) {
    float pq = phi(qkv[s * 1536 + h * 16 + d]);
    num += pq * kv[h * 256 + d * 16 + e];
    den += pq * z[h * 16 + d];
  }
  o[idx] = f2bf(num / (den + 1e-6f));
}

__global__ void k_act(const float* hbuf, const u16* b1, u16* act) {
  int idx = blockIdx.x * blockDim.x + threadIdx.x;
  int f = idx % F;
  float h = hbuf[idx] + bf2f(b1[f]);
  float e = __expf(fminf(h, 20.f));
  float n = e * (e + 2.f);
  act[idx] = f2bf(h * n / (n + 2.f));
}

__global__ void k_out(const u16* x, const u16* gw, const float* a, const float* b, u16* out) {
  int s = blockIdx.x, t = threadIdx.x;
  __shared__ float red[512];
  red[t] = bf2f(x[s * 512 + t]) * bf2f(gw[t]);
  __syncthreads();
  for (int o = 256; o > 0; o >>= 1) { if (t < o) red[t] += red[t + o]; __syncthreads(); }
  float g = 1.f / (1.f + __expf(-red[0]));
  out[s * 512 + t] = f2bf(g * a[s * 512 + t] + (1.f - g) * b[s * 512 + t]);
}

extern "C" void kernel_launch(void* const* d_in, const int* in_sizes, int n_in,
                              void* d_out, int out_size, void* d_ws, size_t ws_size,
                              hipStream_t stream) {
  const u16* x = (const u16*)d_in[0];
  const u16* wqkv = (const u16*)d_in[1];
  const u16* wo = (const u16*)d_in[2];
  const u16* w1 = (const u16*)d_in[4];
  const u16* b1 = (const u16*)d_in[5];
  const u16* w2 = (const u16*)d_in[6];
  const u16* gw = (const u16*)d_in[7];
  char* ws = (char*)d_ws;
  float* qkv = (float*)ws; ws += (size_t)S * 1536 * 4;
  float* hb = (float*)ws; ws += (size_t)S * F * 4;
  float* kv = (float*)ws; ws += 32 * 256 * 4;
  float* z = (float*)ws; ws += 32 * 16 * 4;
  u16* o = (u16*)ws; ws += (size_t)S * D * 2;
  u16* act = (u16*)ws; ws += (size_t)S * F * 2;
  float* a = (float*)ws; ws += (size_t)S * D * 4;
  float* b = (float*)ws; ws += (size_t)S * D * 4;
  k_gemm<<<dim3(1536 / 16, S / 16), dim3(16, 16), 0, stream>>>(x, D, wqkv, 1536, D, qkv);
  k_gemm<<<dim3(F / 16, S / 16), dim3(16, 16), 0, stream>>>(x, D, w1, F, D, hb);
  k_kv<<<32, 256, 0, stream>>>(qkv, kv, z);
  k_o<<<S * D / 256, 256, 0, stream>>>(qkv, kv, z, o);
  k_act<<<S * F / 256, 256, 0, stream>>>(hb, b1, act);
  k_gemm<<<dim3(D / 16, S / 16), dim3(16, 16), 0, stream>>>(o, D, wo, D, D, a);
  k_gemm<<<dim3(D / 16, S / 16), dim3(16, 16), 0, stream>>>(act, F, w2, D, F, b);
  k_out<<<S, 512, 0, stream>>>(x, gw, a, b, (u16*)d_out);
}
```

```cpp
#include <hip/hip_runtime.h>
#include <hip/hip_bf16.h>
#include <stdint.h>

typedef unsigned short u16;
__device__ __forceinline__ float bf2f(u16 b) { return __uint_as_float(((unsigned)b) << 16); }
__device__ __forceinline__ u16 f2bf(float f) { unsigned u = __float_as_uint(f); u += 0x7FFFu + ((u >> 16) & 1u); return (u16)(u >> 16); }

constexpr int S = 2048, D = 512, F = 1792, H = 32, DH = 16;

__global__ void k_gemm(const u16* A, int lda, const u16* W, int N, int K, float* C) {
  __shared__ float As[16][17];
  __shared__ float Ws[16][17];
  int tx = threadIdx.x, ty = threadIdx.y;
  int row = blockIdx.y * 16 + ty, col = blockIdx.x * 16 + tx;
  float acc = 0.f;
  for (int k0 = 0; k0 < K; k0 += 16) {
    As[ty][tx] = bf2f(A[row * lda + k0 + tx]);
    Ws[ty][tx] = bf2f(W[(k0 + ty) * N + col]);
    __syncthreads();
    for (int k = 0; k < 16; ++k) acc += As[ty][k] * Ws[k][tx];
    __syncthreads();
  }
  C[row * N + col] = acc;
}

__device__ __forceinline__ float phi(float t) { return (t > 0.f ? t + 1.f : __expf(t)) + 1e-6f; }

__global__ void k_kv(const float* qkv, float* kv, float* z) {
  int h = blockIdx.x, t = threadIdx.x;
  int d = t >> 4, e = t & 15;
  float acc = 0.f, zz = 0.f;
  for (int s = 0; s < S; ++s) {
    float pk = phi(qkv[s * 1536 + 512 + h * 16 + d]);
    float v = qkv[s * 1536 + 1024 + h * 16 + e];
    acc += pk * v; zz += pk;
  }
  kv[h * 256 + t] = acc;
  if (e == 0) z[h * 16 + d] = zz;
}

__global__ void k_o(const float* qkv, const float* kv, const float* z, u16* o) {
  int idx = blockIdx.x * blockDim.x + threadIdx.x;
  int s = idx >> 9, c = idx & 511, h = c >> 4, e = c & 15;
  float num = 0.f, den = 0.f;
  for (int d = 0; d < 16; ++d) {
    float pq = phi(qkv[s * 1536 + h * 16 + d]);
    num += pq * kv[h * 256 + d * 16 + e];
    den += pq * z[h * 16 + d];
  }
  o[idx] = f2bf(num / (den + 1e-6f));
}

__global__ void k_act(const float* hbuf, const u16* b1, u16* act) {
  int idx = blockIdx.x * blockDim.x + threadIdx.x;
  int f = idx % F;
  float h = hbuf[idx] + bf2f(b1[f]);
  float e = __expf(fminf(h, 20.f));
  float n = e * (e + 2.f);
  act[idx] = f2bf(h * n / (n + 2.f));
}

__global__ void k_out(const u16* x, const u16* gw, const float* a, const float* b, u16* out) {
  int s = blockIdx.x, t = threadIdx.x;
  __shared__ float red[512];
  red[t] = bf2f(x[s * 512 + t]) * bf2f(gw[t]);
  __syncthreads();
  for (int o = 256; o > 0; o >>= 1) { if (t < o) red[t] += red[t + o]; __syncthreads(); }
  float g = 1.f / (1.f + __expf(-red[0]));
  out[s * 512 + t] = f2bf(g * a[s * 512 + t] + (1.f - g) * b[s * 512 + t]);
}


#define R4(x) x x x x
#define R16(x) R4(R4(x))
#define R64(x) R4(R16(x))
#define R256(x) R4(R64(x))
#define R1024(x) R4(R256(x))
__global__ void k_probe(const char* p, int mode, float* sink) {
  unsigned long long lat = 0;
  unsigned v = 0;
  if (mode == 1 || mode == 2) {
    unsigned long long t0 = __builtin_amdgcn_s_memrealtime();
    __builtin_amdgcn_s_waitcnt(0xC07F);
    const char* q = p + 4096;
    asm volatile("global_load_dword %0, %1, off\n\ts_waitcnt vmcnt(0)" : "=v"(v) : "v"(q) : "memory");
    unsigned long long t1 = __builtin_amdgcn_s_memrealtime();
    __builtin_amdgcn_s_waitcnt(0xC07F);
    for (int i = 1; i <= 16; ++i) {
      const char* r = p + 4096 + i * 8320 + (v & 0);
      asm volatile("global_load_dword %0, %1, off\n\ts_waitcnt vmcnt(0)" : "=v"(v) : "v"(r) : "memory");
    }
    unsigned long long t2 = __builtin_amdgcn_s_memrealtime();
    __builtin_amdgcn_s_waitcnt(0xC07F);
    lat = (mode == 1) ? (t1 - t0) * 10 : (t2 - t1) * 10 / 16;
  } else if (mode == 3) {
    unsigned long long t0 = __builtin_amdgcn_s_memrealtime();
    __builtin_amdgcn_s_waitcnt(0xC07F);
    float a = threadIdx.x, b = 1.0001f, c = 0.5f;
    asm volatile(R1024("v_fma_f32 %0, %0, %1, %2\n\t") : "+v"(a) : "v"(b), "v"(c));
    unsigned long long t1 = __builtin_amdgcn_s_memrealtime();
    __builtin_amdgcn_s_waitcnt(0xC07F);
    if (a == 123.456f) sink[0] = a;
    lat = (t1 - t0) * 10 / 4;
  }
  if (lat > 3000) lat = 3000;
  unsigned long long s0 = __builtin_amdgcn_s_memrealtime();
  while (__builtin_amdgcn_s_memrealtime() - s0 < lat * 100) __builtin_amdgcn_s_sleep(8);
  if (v == 0x12345679u) sink[1] = 1.f;
}

extern "C" void kernel_launch(void* const* d_in, const int* in_sizes, int n_in,
                              void* d_out, int out_size, void* d_ws, size_t ws_size,
                              hipStream_t stream) {
  const u16* x = (const u16*)d_in[0];
  const u16* wqkv = (const u16*)d_in[1];
  const u16* wo = (const u16*)d_in[2];
  const u16* w1 = (const u16*)d_in[4];
  const u16* b1 = (const u16*)d_in[5];
  const u16* w2 = (const u16*)d_in[6];
  const u16* gw = (const u16*)d_in[7];
  char* ws = (char*)d_ws;
  float* qkv = (float*)ws; ws += (size_t)S * 1536 * 4;
  float* hb = (float*)ws; ws += (size_t)S * F * 4;
  float* kv = (float*)ws; ws += 32 * 256 * 4;
  float* z = (float*)ws; ws += 32 * 16 * 4;
  u16* o = (u16*)ws; ws += (size_t)S * D * 2;
  u16* act = (u16*)ws; ws += (size_t)S * F * 2;
  float* a = (float*)ws; ws += (size_t)S * D * 4;
  float* b = (float*)ws; ws += (size_t)S * D * 4;
  k_probe<<<1, 64, 0, stream>>>((const char*)x, 0, (float*)ws);
  k_gemm<<<dim3(1536 / 16, S / 16), dim3(16, 16), 0, stream>>>(x, D, wqkv, 1536, D, qkv);
  k_gemm<<<dim3(F / 16, S / 16), dim3(16, 16), 0, stream>>>(x, D, w1, F, D, hb);
  k_kv<<<32, 256, 0, stream>>>(qkv, kv, z);
  k_o<<<S * D / 256, 256, 0, stream>>>(qkv, kv, z, o);
  k_act<<<S * F / 256, 256, 0, stream>>>(hb, b1, act);
  k_gemm<<<dim3(D / 16, S / 16), dim3(16, 16), 0, stream>>>(o, D, wo, D, D, a);
  k_gemm<<<dim3(D / 16, S / 16), dim3(16, 16), 0, stream>>>(act, F, w2, D, F, b);
  k_out<<<S, 512, 0, stream>>>(x, gw, a, b, (u16*)d_out);
}
```

```cpp
#include <hip/hip_runtime.h>
#include <hip/hip_bf16.h>
#include <stdint.h>

typedef unsigned short u16;
__device__ __forceinline__ float bf2f(u16 b) { return __uint_as_float(((unsigned)b) << 16); }
__device__ __forceinline__ u16 f2bf(float f) { unsigned u = __float_as_uint(f); u += 0x7FFFu + ((u >> 16) & 1u); return (u16)(u >> 16); }

constexpr int S = 2048, D = 512, F = 1792, H = 32, DH = 16;

__global__ void k_gemm(const u16* A, int lda, const u16* W, int N, int K, float* C) {
  __shared__ float As[16][17];
  __shared__ float Ws[16][17];
  int tx = threadIdx.x, ty = threadIdx.y;
  int row = blockIdx.y * 16 + ty, col = blockIdx.x * 16 + tx;
  float acc = 0.f;
  for (int k0 = 0; k0 < K; k0 += 16) {
    As[ty][tx] = bf2f(A[row * lda + k0 + tx]);
    Ws[ty][tx] = bf2f(W[(k0 + ty) * N + col]);
    __syncthreads();
    for (int k = 0; k < 16; ++k) acc += As[ty][k] * Ws[k][tx];
    __syncthreads();
  }
  C[row * N + col] = acc;
}

__device__ __forceinline__ float phi(float t) { return (t > 0.f ? t + 1.f : __expf(t)) + 1e-6f; }

__global__ void k_kv(const float* qkv, float* kv, float* z) {
  int h = blockIdx.x, t = threadIdx.x;
  int d = t >> 4, e = t & 15;
  float acc = 0.f, zz = 0.f;
  for (int s = 0; s < S; ++s) {
    float pk = phi(qkv[s * 1536 + 512 + h * 16 + d]);
    float v = qkv[s * 1536 + 1024 + h * 16 + e];
    acc += pk * v; zz += pk;
  }
  kv[h * 256 + t] = acc;
  if (e == 0) z[h * 16 + d] = zz;
}

__global__ void k_o(const float* qkv, const float* kv, const float* z, u16* o) {
  int idx = blockIdx.x * blockDim.x + threadIdx.x;
  int s = idx >> 9, c = idx & 511, h = c >> 4, e = c & 15;
  float num = 0.f, den = 0.f;
  for (int d = 0; d < 16; ++d) {
    float pq = phi(qkv[s * 1536 + h * 16 + d]);
    num += pq * kv[h * 256 + d * 16 + e];
    den += pq * z[h * 16 + d];
  }
  o[idx] = f2bf(num / (den + 1e-6f));
}

__global__ void k_act(const float* hbuf, const u16* b1, u16* act) {
  int idx = blockIdx.x * blockDim.x + threadIdx.x;
  int f = idx % F;
  float h = hbuf[idx] + bf2f(b1[f]);
  float e = __expf(fminf(h, 20.f));
  float n = e * (e + 2.f);
  act[idx] = f2bf(h * n / (n + 2.f));
}

__global__ void k_out(const u16* x, const u16* gw, const float* a, const float* b, u16* out) {
  int s = blockIdx.x, t = threadIdx.x;
  __shared__ float red[512];
  red[t] = bf2f(x[s * 512 + t]) * bf2f(gw[t]);
  __syncthreads();
  for (int o = 256; o > 0; o >>= 1) { if (t < o) red[t] += red[t + o]; __syncthreads(); }
  float g = 1.f / (1.f + __expf(-red[0]));
  out[s * 512 + t] = f2bf(g * a[s * 512 + t] + (1.f - g) * b[s * 512 + t]);
}


typedef unsigned v4u __attribute__((ext_vector_type(4)));
template <int NI, int MODE>
__global__ __launch_bounds__(256) void k_probe(char* p, float* sink) {
  const int lane = threadIdx.x & 63, w = __builtin_amdgcn_readfirstlane(threadIdx.x >> 6);
  __amdgpu_buffer_rsrc_t rs = __builtin_amdgcn_make_buffer_rsrc((void*)p, 0, 64 << 20, 0x00020000);
  unsigned long long t0 = __builtin_amdgcn_s_memrealtime();
  __builtin_amdgcn_s_waitcnt(0xC07F);
  v4u r[NI]; v4u d = {1u, 2u, 3u, 4u};
  const int voff = lane * 16;
#pragma unroll
  for (int i = 0; i < NI; ++i) {
    const int so = (i * 4 + w) * 262144 + blockIdx.x * 1024;
    if (MODE == 0) asm volatile("buffer_load_dwordx4 %0, %1, %2, %3 offen" : "=v"(r[i]) : "v"(voff), "s"(rs), "s"(so) : "memory");
    else { r[i] = d; asm volatile("buffer_store_dwordx4 %0, %1, %2, %3 offen sc1\n\ts_nop 1" :: "v"(d), "v"(voff), "s"(rs), "s"(so) : "memory"); }
  }
  asm volatile("s_waitcnt vmcnt(0)" ::: "memory");
  unsigned long long t1 = __builtin_amdgcn_s_memrealtime();
  __builtin_amdgcn_s_waitcnt(0xC07F);
  unsigned acc = 0;
#pragma unroll
  for (int i = 0; i < NI; ++i) { asm volatile("" : "+v"(r[i])); acc ^= r[i].x; }
  unsigned long long lat = (t1 - t0);
  if (lat > 3000) lat = 3000;
  unsigned long long s0 = __builtin_amdgcn_s_memrealtime();
  while (__builtin_amdgcn_s_memrealtime() - s0 < lat * 100) __builtin_amdgcn_s_sleep(8);
  if (acc == 0x12345679u) sink[1] = 1.f;
}

extern "C" void kernel_launch(void* const* d_in, const int* in_sizes, int n_in,
                              void* d_out, int out_size, void* d_ws, size_t ws_size,
                              hipStream_t stream) {
  const u16* x = (const u16*)d_in[0];
  const u16* wqkv = (const u16*)d_in[1];
  const u16* wo = (const u16*)d_in[2];
  const u16* w1 = (const u16*)d_in[4];
  const u16* b1 = (const u16*)d_in[5];
  const u16* w2 = (const u16*)d_in[6];
  const u16* gw = (const u16*)d_in[7];
  char* ws = (char*)d_ws;
  float* qkv = (float*)ws; ws += (size_t)S * 1536 * 4;
  float* hb = (float*)ws; ws += (size_t)S * F * 4;
  float* kv = (float*)ws; ws += 32 * 256 * 4;
  float* z = (float*)ws; ws += 32 * 16 * 4;
  u16* o = (u16*)ws; ws += (size_t)S * D * 2;
  u16* act = (u16*)ws; ws += (size_t)S * F * 2;
  float* a = (float*)ws; ws += (size_t)S * D * 4;
  float* b = (float*)ws; ws += (size_t)S * D * 4;
  k_probe<1, 1><<<256, 256, 0, stream>>>((char*)d_ws + (128ull << 20), (float*)ws);
  k_gemm<<<dim3(1536 / 16, S / 16), dim3(16, 16), 0, stream>>>(x, D, wqkv, 1536, D, qkv);
  k_gemm<<<dim3(F / 16, S / 16), dim3(16, 16), 0, stream>>>(x, D, w1, F, D, hb);
  k_kv<<<32, 256, 0, stream>>>(qkv, kv, z);
  k_o<<<S * D / 256, 256, 0, stream>>>(qkv, kv, z, o);
  k_act<<<S * F / 256, 256, 0, stream>>>(hb, b1, act);
  k_gemm<<<dim3(D / 16, S / 16), dim3(16, 16), 0, stream>>>(o, D, wo, D, D, a);
  k_gemm<<<dim3(D / 16, S / 16), dim3(16, 16), 0, stream>>>(act, F, w2, D, F, b);
  k_out<<<S, 512, 0, stream>>>(x, gw, a, b, (u16*)d_out);
}
```

```cpp
#include <hip/hip_runtime.h>
#include <stdint.h>

typedef unsigned u32;
typedef unsigned long long u64;
typedef u32 u32x2 __attribute__((ext_vector_type(2)));
typedef u32 u32x4 __attribute__((ext_vector_type(4)));
typedef short s16x4 __attribute__((ext_vector_type(4)));
typedef __bf16 bf16x8 __attribute__((ext_vector_type(8)));
typedef __bf16 bf16x2 __attribute__((ext_vector_type(2)));
typedef float f32x2 __attribute__((ext_vector_type(2)));
typedef float f32x4 __attribute__((ext_vector_type(4)));
typedef float f32x16 __attribute__((ext_vector_type(16)));
typedef __amdgpu_buffer_rsrc_t rsrc_t;
#define LAS __attribute__((address_space(3)))
#define DI __device__ __forceinline__
#define SB() __builtin_amdgcn_sched_barrier(0)

#define EXP 0
namespace {

constexpr u32 WS_E = 0;
constexpr u32 WS_FPROG = 4096;
constexpr u32 WS_FKV = WS_FPROG + 2048 * 128;
constexpr u32 WS_FP = WS_FKV + 256 * 128;
constexpr u32 WS_GATE = WS_FP + 1024 * 128;
constexpr u32 WS_ZP = WS_GATE + 1024 * 128;
constexpr u32 WS_KVP = WS_ZP + 256 * 128;
constexpr u32 WS_OFR = WS_KVP + 256 * 2048;
constexpr u32 WS_ACT = WS_OFR + 2048 * 1024;
constexpr u32 WS_PART = WS_ACT + 512 * 14 * 1024;
constexpr u32 WS_END = WS_PART + 1024 * 4096;
constexpr u32 WS_STAMP = 32u << 20;

constexpr u32 L_R0 = 0, L_R1 = 65536, L_R2 = 98304, L_R3 = 147456, L_END = 163840;
constexpr u32 L_S1 = L_R3;
constexpr u32 L_GW = L_R3 + 13056;
constexpr u32 L_S2 = L_GW + 1024;
static_assert(L_S2 + 2 * 1088 <= L_END, "lds");

constexpr u64 TAG_STEP = 0x9E3779B97F4A7C15ull;
constexpr u32 SPIN_CAP = 1u << 16;
#define CODE_LINES 216

template <int I> struct IC { static constexpr int v = I; };
template <int B, int E, class F> DI void sfor(F&& f) { if constexpr (B < E) { f(IC<B>{}); sfor<B + 1, E>(f); } }
DI u32 fresh_lane() { u32 l; asm volatile("v_mbcnt_lo_u32_b32 %0, -1, 0\n\tv_mbcnt_hi_u32_b32 %0, -1, %0" : "=v"(l)); return l; }
DI f32x16 mfma(u32x4 a, u32x4 b, f32x16 c) {
  return __builtin_amdgcn_mfma_f32_32x32x16_bf16(__builtin_bit_cast(bf16x8, a), __builtin_bit_cast(bf16x8, b), c, 0, 0, 0);
}
DI u32 pk2(float lo, float hi) { f32x2 p = {lo, hi}; return __builtin_bit_cast(u32, __builtin_convertvector(p, bf16x2)); }
DI u32x4 lds128(u32 a) { return *(LAS u32x4*)a; }
DI u32x2 ldstr(u32 a) { return __builtin_bit_cast(u32x2, __builtin_amdgcn_ds_read_tr16_b64_v4i16((LAS s16x4*)a)); }
DI u32x4 ldstr2(u32 a0, u32 a1) { u32x2 lo = ldstr(a0), hi = ldstr(a1); return u32x4{lo.x, lo.y, hi.x, hi.y}; }

template <u32 L, u32 K> DI void dma(u32 wl, u32 voff, rsrc_t rs, u32 sbase) {
  u32 t;
  asm volatile("s_add_u32 m0, %1, %5\n\ts_add_u32 %0, %4, %6\n\tbuffer_load_dwordx4 %2, %3, %0 offen lds"
               : "=&s"(t) : "s"(wl), "v"(voff), "s"(rs), "s"(sbase), "i"(L), "i"(K) : "memory", "scc");
}
template <int OFF> DI void ld128(u32x4& d, u32 voff, rsrc_t rs, u32 soff) {
  asm volatile("buffer_load_dwordx4 %0, %1, %2, %3 offen offset:%4 sc1" : "=v"(d) : "v"(voff), "s"(rs), "s"(soff), "i"(OFF) : "memory");
}
DI void ld64(u32x2& d, u32 voff, rsrc_t rs, u32 soff) {
  asm volatile("buffer_load_dwordx2 %0, %1, %2, %3 offen sc1" : "=v"(d) : "v"(voff), "s"(rs), "s"(soff) : "memory");
}
template <int OFF> DI void ld32(u32& d, u32 voff, rsrc_t rs, u32 soff) {
  asm volatile("buffer_load_dword %0, %1, %2, %3 offen offset:%4 sc1" : "=v"(d) : "v"(voff), "s"(rs), "s"(soff), "i"(OFF) : "memory");
}
template <int OFF> DI void st128(u32x4 d, u32 voff, rsrc_t rs, u32 soff) {
  asm volatile("buffer_store_dwordx4 %0, %1, %2, %3 offen offset:%4 sc1\n\ts_nop 1" ::"v"(d), "v"(voff), "s"(rs), "s"(soff), "i"(OFF) : "memory");
}
DI void st64(u32x2 d, u32 voff, rsrc_t rs, u32 soff) {
  asm volatile("buffer_store_dwordx2 %0, %1, %2, %3 offen sc1" ::"v"(d), "v"(voff), "s"(rs), "s"(soff) : "memory");
}
DI void st32(u32 d, u32 voff, rsrc_t rs, u32 soff) {
  asm volatile("buffer_store_dword %0, %1, %2, %3 offen sc1" ::"v"(d), "v"(voff), "s"(rs), "s"(soff) : "memory");
}
template <int N> DI void waitv() { asm volatile("s_waitcnt vmcnt(%0)" ::"i"(N) : "memory"); }
template <int N> DI void waitv(u32x4& a) { asm volatile("s_waitcnt vmcnt(%1)" : "+v"(a) : "i"(N) : "memory"); }
template <int N> DI void waitv(u32x2& a) { asm volatile("s_waitcnt vmcnt(%1)" : "+v"(a) : "i"(N) : "memory"); }
DI void tie(u32x4& a) { asm volatile("" : "+v"(a)); }
DI void tie(u32x2& a) { asm volatile("" : "+v"(a)); }
DI void tie(u32& a) { asm volatile("" : "+v"(a)); }
DI void tie(f32x16& a) { asm volatile("" : "+v"(a)); }
template <int N> DI void bar_v() { SB(); asm volatile("s_waitcnt vmcnt(%0) lgkmcnt(0)\n\ts_barrier" ::"i"(N) : "memory"); SB(); }
DI void bar_l() { SB(); asm volatile("s_waitcnt lgkmcnt(0)\n\ts_barrier" ::: "memory"); SB(); }

DI bool flag_ok(u32x2 f, u64 tag, u32 need) {
  const u64 d = (((u64)f.y << 32) | f.x) - tag;
  return d >= need && d <= 3;
}
DI void publish(u64 tag, u32 stage, u32 lane, rsrc_t rw, u32 soff) {
  const u64 v = tag + stage;
  st64(u32x2{(u32)v, (u32)(v >> 32)}, lane == 0 ? 0u : 0x80000000u, rw, soff);
}
DI void spin(u32x2& f, u32 voff, rsrc_t rw, u64 tag, u32 need) {
  u32 n = 0;
  while (!__all(need == 0 || flag_ok(f, tag, need))) {
    if (++n > SPIN_CAP) break;
    __builtin_amdgcn_s_sleep(1);
    ld64(f, voff, rw, 0);
    waitv<0>(f);
  }
}

DI float mish(float h) {
  const float e = __builtin_amdgcn_exp2f(__builtin_fmaf(h, 1.44269504f, -0.5f));
  const float p = e + 0.70710678f;
  const float r = __builtin_amdgcn_rcpf(__builtin_fmaf(p, p, 0.5f));
  return __builtin_fmaf(-h, r, h);
}
DI float phi(float t) {
  const float e = __builtin_amdgcn_fmed3f(__builtin_amdgcn_exp2f(t * 1.44269504f), 0.f, 1.f);
  return e + __builtin_fmaxf(t + 1e-6f, 1e-6f);
}

#define STAMP(i) do {} while (0)
#define STAMP_ARG
#define STAMP_PASS

struct Ctx {
  u32 lane, w, m, g, h;
  u32 r1, j, rot;
  u32 r2, ct, kh, b;
  u64 tag;
  rsrc_t rx, rW, rw2, rwo, rw;
  u32 wl;
  u32 sB2, sBo;
  u32 vx, vW, vB;
  u32 ax, aw, aw1, aw2;
};

template <int I> DI void dmaX0(const Ctx& c) {
  dma<L_R0 + I * 8192, (448 * (I >> 2) + ((I >> 1) & 1) * 320 + (I & 1) * 64) * 1024>(c.wl, c.vB, c.rw2, c.sB2);
}
template <int I> DI void dmaX1(const Ctx& c) {
  dma<L_R2 + I * 8192, (448 * (I / 3) + 128 + (I % 3) * 64) * 1024>(c.wl, c.vB, c.rw2, c.sB2);
}
template <int I> DI void dmaX2(const Ctx& c) {
  dma<L_R1 + I * 8192, I * 65536>(c.wl, c.vB, c.rwo, c.sBo);
}

template <int TYPE> struct P1 {
  static constexpr u32 PITCH = TYPE ? 3072 : 3584;
  static constexpr u32 D01 = TYPE ? 1024 : 128;
  u32 sx, s0, sU, sT;
  u32 vT;
  template <int POS, int PART> DI void chunk(const Ctx& c) const {
    const u32 kc = (c.rot + POS) & 7;
    constexpr u32 slot = (POS % 3) * 32768;
    if constexpr (PART < 2) dma<slot + PART * 8192, PART * 65536>(c.wl, c.vx, c.rx, sx + kc * 128);
    else dma<slot + PART * 8192, (PART - 2) * D01>(c.wl, c.vW, c.rW, s0 + kc * (64 * PITCH));
  }
  static constexpr u32 uaddr(int pos) { return (TYPE && pos >= 6) ? L_R1 + (pos - 6) * 8192 : L_R2 + pos * 8192; }
  template <int POS> DI void dmaU(const Ctx& c) const {
    dma<uaddr(POS), 0>(c.wl, c.vW, c.rW, sU + ((c.rot + POS) & 7) * (64 * PITCH));
  }
  template <int PAIR> DI void dmaT(const Ctx& c) const {
    dma<L_R1 + PAIR * 8192, 0>(c.wl, vT, c.rW, sT + ((c.rot + 2 * PAIR + c.g) & 7) * (64 * PITCH));
  }
  template <int IT, int I> DI void later(const Ctx& c) const {
    if constexpr (IT <= 5) chunk<IT + 2, I>(c);
    else if constexpr (TYPE) dmaU<(IT - 6) * 4 + I>(c);
    else if constexpr (I < 2) dmaU<(IT - 6) * 2 + I>(c);
    else if constexpr (I == 2) dmaT<IT - 6>(c);
  }
};

template <int TYPE> DI f32x16 mm1(u32x4 wf, u32x4 xf, f32x16 acc) { return TYPE ? mfma(xf, wf, acc) : mfma(wf, xf, acc); }

DI void rd1(const Ctx& c, int pos, int t, u32x4& xf, u32x4& wa, u32x4& wb) {
  const u32 slot = (pos % 3) * 32768;
  const u32 wbase = (pos % 3 == 2 ? c.aw1 : c.aw + slot) + 16384 + t * 2048;
  xf = lds128((c.ax ^ (t << 5)) + slot);
  wa = ldstr2(wbase, wbase + 512);
  wb = ldstr2(wbase + 8192, wbase + 8192 + 512);
}

template <int TYPE>
DI void pass1(const Ctx& c, const P1<TYPE>& p, u32x4 (&xf)[32], f32x16& aA, f32x16& aB, u32x4 (&wa)[4], u32x4 (&wb)[4] STAMP_ARG) {
  rd1(c, 0, 0, xf[0], wa[0], wb[0]); SB();
  p.template later<0, 0>(c); SB();
  rd1(c, 0, 1, xf[1], wa[1], wb[1]); SB();
  p.template later<0, 1>(c); SB();
  sfor<0, 8>([&](auto I) {
    constexpr int pos = decltype(I)::v;
    rd1(c, pos, 2, xf[4 * pos + 2], wa[2], wb[2]); SB();
    aA = mm1<TYPE>(wa[0], xf[4 * pos], aA); SB();
    aB = mm1<TYPE>(wb[0], xf[4 * pos], aB); SB();
    rd1(c, pos, 3, xf[4 * pos + 3], wa[3], wb[3]); SB();
    p.template later<pos, 2>(c); SB();
    aA = mm1<TYPE>(wa[1], xf[4 * pos + 1], aA); SB();
    p.template later<pos, 3>(c); SB();
    aB = mm1<TYPE>(wb[1], xf[4 * pos + 1], aB); SB();
    if constexpr (pos < 7) {
      bar_v<(pos <= 5 || TYPE) ? 4 : 3>();
      STAMP(16 + pos);
      aA = mm1<TYPE>(wa[2], xf[4 * pos + 2], aA); SB();
      rd1(c, pos + 1, 0, xf[4 * pos + 4], wa[0], wb[0]); SB();
      aB = mm1<TYPE>(wb[2], xf[4 * pos + 2], aB); SB();
      rd1(c, pos + 1, 1, xf[4 * pos + 5], wa[1], wb[1]); SB();
      aA = mm1<TYPE>(wa[3], xf[4 * pos + 3], aA); SB();
      p.template later<pos + 1, 0>(c); SB();
      aB = mm1<TYPE>(wb[3], xf[4 * pos + 3], aB); SB();
      p.template later<pos + 1, 1>(c); SB();
    }
  });
}

DI u32x4 mish8(const f32x16& a, int t) {
  u32x4 r;
#pragma unroll
  for (int i = 0; i < 4; ++i) r[i] = pk2(mish(a[8 * t + 2 * i]), mish(a[8 * t + 2 * i + 1]));
  return r;
}

template <int G>
DI void h_pass2(const Ctx& c, const P1<0>& p, u32x4 (&xf)[32], f32x16& aA, f32x16& aB, u32x4 (&wa)[4], u32x4 (&wb)[4], u32 bias2 STAMP_ARG) {
  const u32 lane = fresh_lane();
  u32 at = L_R1 + (8 * (lane >> 5) + ((lane & 15) >> 2)) * 64 + ((lane >> 4) & 1) * 32 + (lane & 3) * 8;
  tie(at);
  const u32 one = lane >= 32 ? 0u : 0x3F80u;
  const u32x4 bias = {bias2, 0u, 0u, 0u};
  const u32 sact = WS_ACT + ((c.r1 * 4 + c.m) * 8 + c.j) * 14336;
  const u32 sflag = WS_FPROG + ((((c.r1 * 4 + c.m) * 2 + 0) * 8 + c.j) * 2 + G) * 128;
  const u32 vl = lane * 16 + ((EXP & 2) ? 0x80000000u : 0u);
  u32x4 fu[3], ft[3];
  f32x16 aU, aT;
  auto rd2 = [&](int s) {
    const u32 ub = c.aw2 + s * 2048;
    fu[s % 3] = ldstr2(ub, ub + 512);
    if (G == 0) { const u32 tb = at + s * 1024; ft[s % 3] = ldstr2(tb, tb + 256); }
  };
  bar_v<3>();
  STAMP(23);
  aA = mfma(wa[2], xf[30], aA); SB();
  rd2(0); SB();
  aB = mfma(wb[2], xf[30], aB); SB();
  rd2(1); SB();
  aA = mfma(wa[3], xf[31], aA); SB();
  p.dmaU<4>(c); SB();
  aB = mfma(wb[3], xf[31], aB); SB();
  aU = mfma(bias, u32x4{one, 0u, 0u, 0u}, f32x16{}); SB();
  if (G == 0) { aT = mfma(bias, u32x4{one << 16, 0u, 0u, 0u}, f32x16{}); SB(); }
  STAMP(3);
  float e0[4], e1[4];
  u32x4 pk = {};
#pragma unroll
  for (int s = 0; s < 32; ++s) {
    if (s == 6) { bar_v<5>(); STAMP(24); }
    if (s == 14) { bar_v<8>(); STAMP(25); }
    if (s == 22) { bar_v<11>(); STAMP(26); }
    if (s == 30) { bar_l(); STAMP(27); }
    aU = mfma(fu[s % 3], xf[s], aU); SB();
    if (s + 2 < 32) { rd2(s + 2); SB(); }
    if (s < 24 && !(EXP & 4)) {
      const int q = s / 3, ph = s % 3;
      const f32x16& src = q < 4 ? aA : aB;
      const int v0 = (4 * q) & 15;
      if (ph == 0) {
#pragma unroll
        for (int i = 0; i < 4; ++i) e0[i] = __builtin_fmaf(src[v0 + i], 1.44269504f, -0.5f);
#pragma unroll
        for (int i = 0; i < 4; ++i) e0[i] = __builtin_amdgcn_exp2f(e0[i]);
      } else if (ph == 1) {
#pragma unroll
        for (int i = 0; i < 4; ++i) e0[i] += 0.70710678f;
#pragma unroll
        for (int i = 0; i < 4; ++i) e0[i] = __builtin_fmaf(e0[i], e0[i], 0.5f);
        e1[0] = __builtin_amdgcn_rcpf(e0[0]); e1[1] = __builtin_amdgcn_rcpf(e0[1]);
      } else {
        e1[2] = __builtin_amdgcn_rcpf(e0[2]); e1[3] = __builtin_amdgcn_rcpf(e0[3]);
#pragma unroll
        for (int i = 0; i < 4; ++i) e1[i] = __builtin_fmaf(-src[v0 + i], e1[i], src[v0 + i]);
        pk[2 * (q & 1)] = pk2(e1[0], e1[1]); pk[2 * (q & 1) + 1] = pk2(e1[2], e1[3]);
      }
      SB();
    }
    if (G == 0) { aT = mfma(ft[s % 3], xf[s], aT); SB(); }
    if (s == 0) p.dmaU<5>(c);
    if (s == 1) p.dmaT<2>(c);
    if (s == 3) p.dmaU<6>(c);
    if (s == 5) p.dmaU<7>(c);
    if (s == 6) { p.dmaT<3>(c); st128<0>(pk, vl, c.rw, sact + G * 4096); }
    if (s == 8) dmaX0<0>(c);
    if (s == 10) dmaX0<1>(c);
    if (s == 11) { dmaX0<2>(c); st128<1024>(pk, vl, c.rw, sact + G * 4096); }
    if (s == 14) dmaX0<3>(c);
    if (s == 15) dmaX0<4>(c);
    if (s == 17) { dmaX0<5>(c); st128<2048>(pk, vl, c.rw, sact + G * 4096); }
    if (s == 19) dmaX0<6>(c);
    if (s == 20) dmaX0<7>(c);
    if (s == 22) dmaX1<0>(c);
    if (s == 23) { dmaX1<1>(c); st128<3072>(pk, vl, c.rw, sact + G * 4096); }
    if (s == 25) dmaX1<2>(c);
    if (s == 26) dmaX1<3>(c);
    if (s == 28) dmaX1<4>(c);
    if (s == 29) dmaX1<5>(c);
    if (s == 31) dmaX2<0>(c);
    SB();
  }
  STAMP(4);
  pk = mish8(aU, 0); SB();
  dmaX2<1>(c); SB();
  waitv<6>();
  publish(c.tag, 1, lane, c.rw, sflag); SB();
  st128<0>(pk, vl, c.rw, sact + 8192 + G * 2048); SB();
  dmaX2<2>(c); SB();
  pk = mish8(aU, 1); SB();
  st128<1024>(pk, vl, c.rw, sact + 8192 + G * 2048); SB();
  dmaX2<3>(c); SB();
  if (G == 0) {
    pk = mish8(aT, 0); SB();
    st128<0>(pk, vl, c.rw, sact + 12288); SB();
    pk = mish8(aT, 1); SB();
    st128<1024>(pk, vl, c.rw, sact + 12288); SB();
  }
}

template <int G>
DI void gate_half(const Ctx& c, u32x4 (&xf)[32], const f32x16& aQ, f32x16& aG, u32x4& pq0, u32x4& pq1) {
  u32x4 gf[3];
  auto rdg = [&](int s) { gf[s % 3] = lds128(L_GW + ((c.rot + 4 * G + (s >> 2)) & 7) * 128 + (s & 3) * 32 + c.h * 16); };
  rdg(0); rdg(1); SB();
  sfor<0, 16>([&](auto I) {
    constexpr int i = decltype(I)::v;
    if constexpr (i + 2 < 16) rdg(i + 2);
    if constexpr (i < 4) pq0[i] = pk2(phi(aQ[2 * i]), phi(aQ[2 * i + 1]));
    else if constexpr (i < 8) pq1[i - 4] = pk2(phi(aQ[2 * i]), phi(aQ[2 * i + 1]));
    SB();
    aG = mfma(gf[i % 3], xf[16 * G + i], aG); SB();
    if constexpr ((i & 1) == 0) { if constexpr (i < 12) dmaX1<i / 2>(c); else dmaX2<(i - 12) / 2>(c); SB(); }
  });
  tie(aG); tie(pq0); tie(pq1);
}

DI void qkv_pass2(Ctx& c, u32x4 (&xf)[32], f32x16& aA, f32x16& aB, u32x4 (&wa)[4], u32x4 (&wb)[4] STAMP_ARG) {
  const u32 lane = fresh_lane(), vl = lane * 16;
  c.lane = lane; c.h = lane >> 5;
  aA = mfma(xf[30], wa[2], aA); SB();
  aB = mfma(xf[30], wb[2], aB); SB();
  aA = mfma(xf[31], wa[3], aA); SB();
  aB = mfma(xf[31], wb[3], aB); SB();
  STAMP(3);
  u32x4 pk0, pk1, v0, v1;
#pragma unroll
  for (int i = 0; i < 4; ++i) pk0[i] = pk2(phi(aA[2 * i]), phi(aA[2 * i + 1]));
  SB(); dmaX0<0>(c); SB();
#pragma unroll
  for (int i = 0; i < 4; ++i) pk1[i] = pk2(phi(aA[8 + 2 * i]), phi(aA[9 + 2 * i]));
  SB(); dmaX0<1>(c); SB();
#pragma unroll
  for (int i = 0; i < 4; ++i) { v0[i] = pk2(aB[2 * i], aB[2 * i + 1]); v1[i] = pk2(aB[8 + 2 * i], aB[9 + 2 * i]); }
  const u32x4 ones = {0x3F803F80u, 0x3F803F80u, 0x3F803F80u, 0x3F803F80u};
  f32x16 R = mfma(pk0, v0, f32x16{});
  f32x16 Z = mfma(pk0, ones, f32x16{});
  R = mfma(pk1, v1, R);
  Z = mfma(pk1, ones, Z);
  SB(); dmaX0<2>(c); SB(); dmaX0<3>(c); SB();
  const bool hi = (lane & 16) != 0;
  u32 him = hi ? 0xFFFFFFFFu : 0u;
  tie(him);
  auto sel = [&](float a, float b) { return __builtin_bit_cast(float, (__builtin_bit_cast(u32, a) & him) | (__builtin_bit_cast(u32, b) & ~him)); };
  f32x4 s0, s1, z0, z1;
#pragma unroll
  for (int i = 0; i < 4; ++i) {
    s0[i] = sel(R[8 + i], R[i]); s1[i] = sel(R[12 + i], R[4 + i]);
    z0[i] = sel(Z[8 + i], Z[i]); z1[i] = sel(Z[12 + i], Z[4 + i]);
  }
  const u32 zoff = (((lane >> 4) & 1) * 2 + c.h) * 32;
  if (c.m != 0) {
    const u32 sb = L_S1 + (c.g * 3 + c.m - 1) * 2176;
    *(LAS f32x4*)(sb + vl) = s0;
    *(LAS f32x4*)(sb + 1024 + vl) = s1;
    if ((lane & 15) == 0) { *(LAS f32x4*)(sb + 2048 + zoff) = z0; *(LAS f32x4*)(sb + 2048 + zoff + 16) = z1; }
  }
  STAMP(23);
  bar_v<4>();
  STAMP(24);
  const u32 kvrec = (c.j * 2 + c.g) * 16;
  if (c.m == 0) {
#pragma unroll
    for (int mm = 0; mm < 3; ++mm) {
      const u32 sb = L_S1 + (c.g * 3 + mm) * 2176;
      s0 += *(LAS f32x4*)(sb + vl); s1 += *(LAS f32x4*)(sb + 1024 + vl);
      z0 += *(LAS f32x4*)(sb + 2048 + zoff); z1 += *(LAS f32x4*)(sb + 2048 + zoff + 16);
    }
    const u32 vz = (lane & 15) == 0 ? zoff : 0x80000000u;
    st128<0>(__builtin_bit_cast(u32x4, s0), vl, c.rw, WS_KVP + (kvrec + c.r1) * 2048);
    st128<1024>(__builtin_bit_cast(u32x4, s1), vl, c.rw, WS_KVP + (kvrec + c.r1) * 2048);
    st128<0>(__builtin_bit_cast(u32x4, z0), vz, c.rw, WS_ZP + (kvrec + c.r1) * 128);
    st128<16>(__builtin_bit_cast(u32x4, z1), vz, c.rw, WS_ZP + (kvrec + c.r1) * 128);
  }
  SB();
  STAMP(4);
  u32x4 fu[3];
  auto rdq = [&](int s) { const u32 ub = s < 24 ? c.aw2 + s * 2048 : c.aw1 + (s - 24) * 2048; fu[s % 3] = ldstr2(ub, ub + 512); };
  rdq(0); rdq(1); SB();
  f32x16 aQ = {};
  u32x2 fl;
  const u32 vfl = WS_FKV + (kvrec + (lane & 15)) * 128;
#pragma unroll
  for (int s = 0; s < 32; ++s) {
    aQ = mfma(fu[s % 3], xf[s], aQ); SB();
    if (s + 2 < 32) { rdq(s + 2); SB(); }
    if (s == 0) dmaX0<4>(c);
    if (s == 2) dmaX0<5>(c);
    if (s == 4) dmaX0<6>(c);
    if (s == 6) dmaX0<7>(c);
    if (s == 8) dmaX2<2>(c);
    if (s == 10) dmaX2<3>(c);
    if (s == 13) { tie(aQ); STAMP(25); }
    if (s == 14) STAMP(26);
    if (s == 13 && c.m == 0) { waitv<6>(); publish(c.tag, 1, lane, c.rw, WS_FKV + (kvrec + c.r1) * 128); }
    if (s == 24) ld64(fl, vfl, c.rw, 0);
    SB();
  }
  tie(aQ);
  STAMP(27);
  waitv<0>(fl);
  STAMP(28);
  spin(fl, vfl, c.rw, c.tag, 1);
  STAMP(5);
  u32x4 pr[8], pz[2];
  const u32 vp = c.h * 2048 + c.m * 512 + (lane & 31) * 16;
  if (c.m == 3) { ld128<0>(pz[0], vl, c.rw, WS_ZP + kvrec * 128); ld128<1024>(pz[1], vl, c.rw, WS_ZP + kvrec * 128); }
#pragma unroll
  for (int i = 0; i < 8; ++i) ld128<0>(pr[i], vp, c.rw, WS_KVP + (kvrec + 2 * i) * 2048);
  bar_l();
  f32x16 aG = {};
  u32x4 pq0, pq1;
  if (c.g) gate_half<1>(c, xf, aQ, aG, pq0, pq1); else gate_half<0>(c, xf, aQ, aG, pq0, pq1);
  waitv<8>();
#pragma unroll
  for (int i = 0; i < 8; ++i) tie(pr[i]);
  STAMP(6);
  f32x4 t = __builtin_bit_cast(f32x4, pr[0]);
#pragma unroll
  for (int i = 1; i < 8; ++i) t += __builtin_bit_cast(f32x4, pr[i]);
#pragma unroll
  for (int i = 0; i < 4; ++i) t[i] += __shfl_xor(t[i], 32);
  const u32 s2 = L_S2 + c.g * 1088;
  if (lane < 32) *(LAS u32x2*)(s2 + (lane + 32 * (c.m & 1)) * 16 + (c.m >> 1) * 8) = u32x2{pk2(t[0], t[1]), pk2(t[2], t[3])};
  if (c.m == 3) {
    tie(pz[0]); tie(pz[1]);
    f32x4 u = __builtin_bit_cast(f32x4, pz[0]) + __builtin_bit_cast(f32x4, pz[1]);
#pragma unroll
    for (int i = 0; i < 4; ++i) { u[i] += __shfl_xor(u[i], 8); u[i] += __shfl_xor(u[i], 16); u[i] += __shfl_xor(u[i], 32); }
    if (lane < 8) *(LAS u32x2*)(s2 + 1024 + lane * 8) = u32x2{pk2(u[0], u[1]), pk2(u[2], u[3])};
  }
  bar_l();
  const u32x4 A0 = lds128(hi ? s2 + 1024 + c.h * 16 : s2 + vl);
  const u32x4 A1 = lds128(hi ? s2 + vl : s2 + 1024 + 32 + c.h * 16);
  const f32x16 Y0 = mfma(A0, pq0, f32x16{});
  const f32x16 Y1 = mfma(A1, pq1, f32x16{});
  const float i0 = __builtin_amdgcn_rcpf(Y0[8] + 1e-6f), i1 = __builtin_amdgcn_rcpf(Y1[0] + 1e-6f);
  u32x4 o0, o1;
#pragma unroll
  for (int i = 0; i < 4; ++i) { o0[i] = pk2(Y0[2 * i] * i0, Y0[2 * i + 1] * i0); o1[i] = pk2(Y1[8 + 2 * i] * i1, Y1[9 + 2 * i] * i1); }
  const u32 orec = (c.r1 * 4 + c.m) * 8 + c.j;
  st128<0>(o0, vl, c.rw, WS_OFR + orec * 4096 + c.g * 2048);
  st128<1024>(o1, vl, c.rw, WS_OFR + orec * 4096 + c.g * 2048);
  st32(__builtin_bit_cast(u32, aG[0]), lane < 32 ? lane * 4 : 0x80000000u, c.rw, WS_GATE + (orec * 2 + c.g) * 128);
  SB();
}

DI constexpr u32 bblock(int s) {
  if (s < 16) { const int jj = s >> 3, fi = s & 7; return jj * 16384 + ((fi >> 1) & 1) * 8192 + (fi >> 2) * 4096 + (fi & 1) * 2048; }
  if (s < 28) { const int jj = (s - 16) / 6, fi = (s - 16) % 6;
    return jj * 12288 + (jj == 0 ? fi * 2048 : (fi < 4 ? 4096 + fi * 2048 : (fi - 4) * 2048)); }
  return (s - 28) * 2048;
}

}

__global__ __launch_bounds__(512, 2) void gated_mix_kernel(const void* x, const void* wqkv, const void* wo,
                                                           const void* w1, const void* b1, const void* w2,
                                                           const void* gw, void* out, void* ws) {
  __shared__ __attribute__((aligned(1024))) char smem[L_END];
  STAMP(0);
  u32 pf;
  {
    u64 pc; asm volatile("s_getpc_b64 %0" : "=s"(pc));
    const char* pp = (const char*)(pc & ~127ull) + (threadIdx.x < CODE_LINES ? threadIdx.x : 0) * 128;
    asm volatile("global_load_dword %0, %1, off" : "=v"(pf) : "v"(pp) : "memory");
  }
  Ctx c;
  u32 lane = threadIdx.x & 63;
  c.lane = lane; c.h = lane >> 5;
  c.w = __builtin_amdgcn_readfirstlane(threadIdx.x >> 6); c.m = c.w & 3; c.g = c.w >> 2;
  const u32 b = blockIdx.x, xc = b & 7, slot = b >> 3;
  const u32 type = slot >> 4;
  c.b = b;
  c.r1 = (xc >> 2) * 8 + (slot & 7); c.j = 2 * (xc & 3) + ((slot >> 3) & 1);
  c.rot = ((slot & 7) + 2 * (slot >> 3) + (xc & 1)) & 7;
  c.r2 = (xc >> 1) * 4 + (slot & 3); c.ct = slot >> 2; c.kh = xc & 1;
  const u32 lbase = (u32)(uintptr_t)(LAS char*)smem;
  c.wl = lbase + c.w * 1024;
  c.sB2 = (896 * c.kh + 8 * c.w) * 1024 + c.ct * 128;
  c.sBo = (256 * c.kh + 8 * c.w) * 1024 + c.ct * 128;
  const u32 lrow = lane >> 3, lpos = lane & 7;
  c.vx = lrow * 1024 + ((lpos ^ ((4 * (c.w & 1) + (lane >> 4)) & 7)) * 16);
  const u32 wsw = (lpos ^ (((lane >> 4) & 1) << 2)) * 16;
  c.vB = lrow * 1024 + wsw;
  {
    const u32 row = 32 * c.m + (lane & 31);
    c.ax = lbase + row * 128 + ((c.h ^ ((row >> 1) & 7)) * 16);
    const u32 q = (lane & 15) >> 2, pp = lane & 3, blk = (lane >> 4) & 1;
    c.aw = lbase + (8 * c.h + q) * 128 + (((4 * c.g + 2 * blk + (pp >> 1)) ^ ((q >> 1) << 2)) * 16) + 8 * (pp & 1);
    c.aw1 = c.aw + L_R1; c.aw2 = c.aw + L_R2;
    tie(c.aw1); tie(c.aw2);
  }
  asm volatile("" : "+s"(x), "+s"(wqkv), "+s"(wo), "+s"(w1), "+s"(b1), "+s"(w2), "+s"(gw), "+s"(out), "+s"(ws));
  c.rx = __builtin_amdgcn_make_buffer_rsrc((void*)x, 0, 2048 * 1024, 0x00020000);
  c.rw2 = __builtin_amdgcn_make_buffer_rsrc((void*)w2, 0, (EXP & 1) ? 0 : 1792 * 1024, 0x00020000);
  c.rwo = __builtin_amdgcn_make_buffer_rsrc((void*)wo, 0, (EXP & 1) ? 0 : 512 * 1024, 0x00020000);
  c.rw = __builtin_amdgcn_make_buffer_rsrc(ws, 0, WS_END, 0x00020000);
  u32x2 ep;

  u32x4 xf[32], wa[4], wb[4];
  if (type == 0) {
    c.rW = __builtin_amdgcn_make_buffer_rsrc((void*)w1, 0, 512 * 3584, 0x00020000);
    c.vW = lrow * 3584 + wsw;
    P1<0> p;
    const u32 odd = c.j & 1, c0 = 448 * c.j;
    const u32 cu0 = c0 + (odd ? 192 : 0), cuU = c0 + (odd ? 64 : 256), cuT = c0 + (odd ? 0 : 384);
    p.sx = (c.r1 * 128 + 8 * c.w) * 1024;
    p.s0 = 8 * c.w * 3584 + cu0; p.sU = 8 * c.w * 3584 + cuU; p.sT = 16 * c.m * 3584 + cuT;
    p.vT = (lane >> 2) * 3584 + (lane & 3) * 16;
    sfor<0, 4>([&](auto I) { p.chunk<0, decltype(I)::v>(c); });
    ld64(ep, 0u, c.rw, WS_E);
    const rsrc_t rb = __builtin_amdgcn_make_buffer_rsrc((void*)b1, 0, 3584, 0x00020000);
    u32 bA, bB, bU, bT;
    const u32 vb = (lane & 31) * 2;
    asm volatile("buffer_load_ushort %0, %4, %5, %6 offen\n\tbuffer_load_ushort %1, %4, %5, %6 offen offset:128\n\t"
                 "buffer_load_ushort %2, %4, %5, %7 offen\n\tbuffer_load_ushort %3, %4, %5, %8 offen"
                 : "=&v"(bA), "=&v"(bB), "=&v"(bU), "=&v"(bT) : "v"(vb), "s"(rb), "s"(cu0 + 64 * c.g), "s"(cuU + 64 * c.g), "s"(cuT) : "memory");
    sfor<0, 4>([&](auto I) { p.chunk<1, decltype(I)::v>(c); });
    STAMP(1);
    bar_v<4>();
    STAMP(2);
    tie(ep); tie(pf); tie(bA); tie(bB); tie(bU); tie(bT);
    c.tag = (((u64)(u32)__builtin_amdgcn_readfirstlane(ep.y) << 32) | (u32)__builtin_amdgcn_readfirstlane(ep.x)) + TAG_STEP;
    const u32x4 bias = {bA | (bB << 16), 0u, 0u, 0u};
    u32 bias2 = bU | (bT << 16);
    tie(bias2);
    const u32 one = c.h ? 0u : 0x3F80u;
    f32x16 aA = mfma(bias, u32x4{one, 0u, 0u, 0u}, f32x16{});
    f32x16 aB = mfma(bias, u32x4{one << 16, 0u, 0u, 0u}, f32x16{});
    SB();
    pass1<0>(c, p, xf, aA, aB, wa, wb STAMP_PASS);
    tie(aA); tie(aB);
    if (c.g == 0) h_pass2<0>(c, p, xf, aA, aB, wa, wb, bias2 STAMP_PASS);
    else h_pass2<1>(c, p, xf, aA, aB, wa, wb, bias2 STAMP_PASS);
  } else {
    c.rW = __builtin_amdgcn_make_buffer_rsrc((void*)wqkv, 0, 512 * 3072, 0x00020000);
    c.vW = lrow * 3072 + wsw;
    P1<1> p;
    p.sx = (c.r1 * 128 + 8 * c.w) * 1024;
    p.sU = 8 * c.w * 3072 + 128 * c.j; p.s0 = p.sU + 1024; p.sT = 0; p.vT = 0;
    sfor<0, 4>([&](auto I) { p.chunk<0, decltype(I)::v>(c); });
    ld64(ep, 0u, c.rw, WS_E);
    if (c.w == 0) {
      const rsrc_t rg = __builtin_amdgcn_make_buffer_rsrc((void*)gw, 0, 1024, 0x00020000);
      dma<L_GW, 0>(c.wl, lane * 16, rg, 0u);
    }
    sfor<0, 4>([&](auto I) { p.chunk<1, decltype(I)::v>(c); });
    STAMP(1);
    bar_v<4>();
    STAMP(2);
    tie(ep); tie(pf);
    c.tag = (((u64)(u32)__builtin_amdgcn_readfirstlane(ep.y) << 32) | (u32)__builtin_amdgcn_readfirstlane(ep.x)) + TAG_STEP;
    f32x16 aA = {}, aB = {};
    pass1<1>(c, p, xf, aA, aB, wa, wb STAMP_PASS);
    qkv_pass2(c, xf, aA, aB, wa, wb STAMP_PASS);
  }
  STAMP(7);

  lane = fresh_lane(); c.lane = lane; c.h = lane >> 5;
  const u32 mrow = c.m & 1, kq = c.m >> 1, q4 = 2 * c.kh + kq;
  const u32 rt = 2 * (c.kh ^ c.g) + mrow;
  const u32 vl = lane * 16;
  const u32 vfl = WS_FPROG + (((((c.r2 * 4 + rt) * 2 + ((lane >> 2) & 1)) * 8 + 2 * q4) * 2) + (lane & 3)) * 128;
  const bool isH = (lane & 4) == 0;
  u32x2 fl;
  ld64(fl, vfl, c.rw, 0);
  waitv<0>(fl);
  publish(c.tag, 2, lane, c.rw, WS_FPROG + ((((c.r1 * 4 + c.m) * 2 + type) * 8 + c.j) * 2 + c.g) * 128);
  STAMP(8);
  spin(fl, vfl, c.rw, c.tag, isH ? 1 : 0);
  const u32 sact = WS_ACT + ((c.r2 * 4 + rt) * 8 + 2 * q4) * 14336;
  const u32 sofr = WS_OFR + ((c.r2 * 4 + rt) * 8 + 2 * q4) * 4096;
  u32x4 a[36];
#define LDA4(i, so) ld128<0>(a[i], vl, c.rw, so); ld128<1024>(a[i + 1], vl, c.rw, so); ld128<2048>(a[i + 2], vl, c.rw, so); ld128<3072>(a[i + 3], vl, c.rw, so);
  LDA4(0, sact) LDA4(4, sact + 4096) LDA4(8, sact + 14336) LDA4(12, sact + 14336 + 4096)
  ld64(fl, vfl, c.rw, 0);
  STAMP(9);
  bar_l();
  if (c.g) __builtin_amdgcn_s_setprio(1);
  STAMP(10);
  u32 ab[3][2];
  {
    const u32 q = (lane & 15) >> 2, pp = lane & 3, blk = (lane >> 4) & 1;
    const u32 a0 = lbase + (4 * c.h + q) * 128 + (((4 * kq + 2 * blk + (pp >> 1)) ^ ((q >> 1) << 2)) * 16) + 8 * (pp & 1);
    ab[0][0] = a0 + L_R0 + kq * 32768; ab[1][0] = a0 + L_R2 + kq * 24576; ab[2][0] = a0 + L_R1 + kq * 16384;
#pragma unroll
    for (int i = 0; i < 3; ++i) { ab[i][1] = ab[i][0] ^ 64; tie(ab[i][0]); tie(ab[i][1]); }
  }
  u32x4 f0[3], f1[3];
  auto rdb = [&](int s) {
    const int sec = s < 16 ? 0 : s < 28 ? 1 : 2;
    f0[s % 3] = ldstr2(ab[sec][0] + bblock(s), ab[sec][0] + bblock(s) + 1024);
    f1[s % 3] = ldstr2(ab[sec][1] + bblock(s), ab[sec][1] + bblock(s) + 1024);
  };
  rdb(0); rdb(1); SB();
  f32x16 cB0 = {}, cB1 = {}, cA0 = {}, cA1 = {};
  u32 gl0, gl1;
  u32x2 fp;
  const u32 spart_in = WS_PART + ((c.b ^ 1) * 4 + c.m) * 4096, sfp_in = WS_FP + ((c.b ^ 1) * 4 + c.m) * 128;
  auto step = [&](auto I) {
    constexpr int S = decltype(I)::v;
    waitv<(S < 16 ? 16 : S < 28 ? 28 : 36) - S>(a[S]); SB();
    if constexpr (S < 28) { cB0 = mfma(f0[S % 3], a[S], cB0); SB(); cB1 = mfma(f1[S % 3], a[S], cB1); SB(); }
    else { cA0 = mfma(f0[S % 3], a[S], cA0); SB(); cA1 = mfma(f1[S % 3], a[S], cA1); SB(); }
    if constexpr (S + 2 < 36) { rdb(S + 2); SB(); }
  };
  sfor<0, 16>(step);
  tie(cB0); tie(cB1);
  waitv<0>(fl);
  STAMP(11);
  spin(fl, vfl, c.rw, c.tag, isH ? 2 : 0);
  LDA4(16, sact + 8192) ld128<0>(a[20], vl, c.rw, sact + 12288); ld128<1024>(a[21], vl, c.rw, sact + 12288);
  LDA4(22, sact + 14336 + 8192) ld128<0>(a[26], vl, c.rw, sact + 14336 + 12288); ld128<1024>(a[27], vl, c.rw, sact + 14336 + 12288);
  ld64(fl, vfl, c.rw, 0);
  SB();
  sfor<16, 28>(step);
  tie(cB0); tie(cB1);
  waitv<0>(fl);
  STAMP(12);
  spin(fl, vfl, c.rw, c.tag, 2);
  {
    const u32 sg = WS_GATE + ((c.r2 * 4 + rt) * 8 + 2 * q4) * 256;
    ld32<0>(gl0, (lane & 31) * 4, c.rw, sg); ld32<128>(gl1, (lane & 31) * 4, c.rw, sg);
  }
  LDA4(28, sofr) LDA4(32, sofr + 4096)
  ld64(fp, sfp_in, c.rw, 0);
  SB();
  sfor<28, 36>(step);
  tie(gl0); tie(gl1);
  const float gate = __builtin_amdgcn_rcpf(1.f + __builtin_amdgcn_exp2f(-1.44269504f * (__builtin_bit_cast(float, gl0) + __builtin_bit_cast(float, gl1))));
#pragma unroll
  for (int i = 0; i < 16; ++i) { cB0[i] += gate * (cA0[i] - cB0[i]); cB1[i] += gate * (cA1[i] - cB1[i]); }
  STAMP(13);
  if (c.g) {
    const u32 so = WS_PART + (c.b * 4 + c.m) * 4096;
    u32x4 t;
#define PK8(acc, o) for (int i = 0; i < 4; ++i) t[i] = pk2(acc[o + 2 * i], acc[o + 2 * i + 1]);
    PK8(cB0, 0) st128<0>(t, vl, c.rw, so);
    PK8(cB0, 8) st128<1024>(t, vl, c.rw, so);
    PK8(cB1, 0) st128<2048>(t, vl, c.rw, so);
    PK8(cB1, 8) st128<3072>(t, vl, c.rw, so);
    const bool bump = c.b == 0 && c.w == 4;
    const u32 ve = WS_FPROG + ((lane >> 4) * 128 + (lane & 15) * 2) * 128;
    u32x2 fe[4];
    if (bump) {
#pragma unroll
      for (int i = 0; i < 4; ++i) ld64(fe[i], ve + i * 65536, c.rw, 0);
    }
    waitv<0>(fp);
    publish(c.tag, 1, lane, c.rw, WS_FP + (c.b * 4 + c.m) * 128);
    STAMP(14);
    if (bump) {
#pragma unroll
      for (int i = 0; i < 4; ++i) { tie(fe[i]); spin(fe[i], ve + i * 65536, c.rw, c.tag, 1); }
      publish(c.tag, 0, lane, c.rw, WS_E);
    }
  } else {
    waitv<0>(fp);
    spin(fp, sfp_in, c.rw, c.tag, 1);
    u32x4 pp[4];
    ld128<0>(pp[0], vl, c.rw, spart_in); ld128<1024>(pp[1], vl, c.rw, spart_in); ld128<2048>(pp[2], vl, c.rw, spart_in); ld128<3072>(pp[3], vl, c.rw, spart_in);
    waitv<0>();
    tie(pp[0]); tie(pp[1]); tie(pp[2]); tie(pp[3]);
    STAMP(14);
#pragma unroll
    for (int i = 0; i < 8; ++i) {
      const u32 u0 = pp[i >> 2][i & 3], u1 = pp[2 + (i >> 2)][i & 3];
      cB0[2 * i] += __builtin_bit_cast(float, u0 << 16); cB0[2 * i + 1] += __builtin_bit_cast(float, u0 & 0xFFFF0000u);
      cB1[2 * i] += __builtin_bit_cast(float, u1 << 16); cB1[2 * i + 1] += __builtin_bit_cast(float, u1 & 0xFFFF0000u);
    }
    const u32 sc = lbase + L_R3 + c.m * 4096 + vl, sr = lbase + L_R3 + (c.m ^ 2) * 4096 + vl;
    f32x16 keep = cB0;
#pragma unroll
    for (int i = 0; i < 4; ++i) *(LAS f32x4*)(sc + i * 1024) = f32x4{cB1[4 * i], cB1[4 * i + 1], cB1[4 * i + 2], cB1[4 * i + 3]};
    bar_l();
#pragma unroll
    for (int i = 0; i < 4; ++i) {
      const f32x4 v = *(LAS f32x4*)(sr + i * 1024);
#pragma unroll
      for (int k = 0; k < 4; ++k) keep[4 * i + k] += v[k];
    }
    const rsrc_t ro = __builtin_amdgcn_make_buffer_rsrc(out, 0, 2048 * 1024, 0x00020000);
    const u32 vo = (lane & 31) * 1024 + c.h * 16, so = (c.r2 * 128 + rt * 32) * 1024 + c.ct * 128 + kq * 64;
#pragma unroll
    for (int i = 0; i < 2; ++i) {
      u32 a0 = pk2(keep[8 * i], keep[8 * i + 1]), a1 = pk2(keep[8 * i + 2], keep[8 * i + 3]);
      u32 b0 = pk2(keep[8 * i + 4], keep[8 * i + 5]), b1 = pk2(keep[8 * i + 6], keep[8 * i + 7]);
      const auto r0 = __builtin_amdgcn_permlane32_swap(a0, b0, false, false);
      const auto r1 = __builtin_amdgcn_permlane32_swap(a1, b1, false, false);
      __builtin_amdgcn_raw_buffer_store_b128(u32x4{r0[0], r1[0], r0[1], r1[1]}, ro, vo + i * 32, so, 0);
    }
  }
}

extern "C" void kernel_launch(void* const* d_in, const int* in_sizes, int n_in, void* d_out, int out_size, void* d_ws, size_t ws_size,
                              hipStream_t stream) {
  gated_mix_kernel<<<256, 512, 0, stream>>>(d_in[0], d_in[1], d_in[2], d_in[4], d_in[5], d_in[6], d_in[7], d_out, d_ws);
}
```

```cpp
#include <hip/hip_runtime.h>
#include <stdint.h>

typedef unsigned u32;
typedef unsigned long long u64;
typedef u32 u32x2 __attribute__((ext_vector_type(2)));
typedef u32 u32x4 __attribute__((ext_vector_type(4)));
typedef short s16x4 __attribute__((ext_vector_type(4)));
typedef __bf16 bf16x8 __attribute__((ext_vector_type(8)));
typedef float f32x4 __attribute__((ext_vector_type(4)));
typedef float f32x16 __attribute__((ext_vector_type(16)));
typedef u64 u64x4 __attribute__((ext_vector_type(4)));
typedef u64 u64x8 __attribute__((ext_vector_type(8)));
typedef __amdgpu_buffer_rsrc_t rsrc_t;
#define LAS __attribute__((address_space(3)))
#define DI __device__ __forceinline__
#define SB() __builtin_amdgcn_sched_barrier(0)

#define EXP 0
#define ROTA 0
#define ROTB 0
#define ROTC 2
#define ROTD 4
#define SPINSLEEP 2
#define PD 3
#define PFMODE 1
#define CODE_LINES 224

namespace {

constexpr u32 WS_E = 0;
constexpr u32 WS_FPROG = 4096;
constexpr u32 WS_FKV = WS_FPROG + 256 * 128;
constexpr u32 WS_FP = WS_FKV + 64 * 128;
constexpr u32 WS_GATE = WS_FP + 1024 * 128;
constexpr u32 WS_ZP = WS_GATE + 1024 * 128;
constexpr u32 WS_KVP = WS_ZP + 256 * 128;
constexpr u32 WS_HND = WS_KVP + 256 * 2048;
constexpr u32 WS_PART = (WS_HND + 256 * 36864 + 32767) & ~32767u;
constexpr u32 WS_END = WS_PART + 1024 * 4096;
constexpr u32 WS_STAMP = 32u << 20;

constexpr u32 L_R0 = 0, L_R1 = 65536, L_R2 = 98304, L_R3 = 147456, L_END = 163840;
constexpr int NSLOT = PD + 2;
constexpr u32 RING_SLOT = 24576, L_RING = 139264 - NSLOT * RING_SLOT;
DI constexpr u32 ring(int pos) { return L_RING + ((pos + (PD == 3 ? 2 : 0)) % NSLOT) * RING_SLOT; }
constexpr u32 L_S1 = L_R0;
constexpr u32 L_GW = L_R3;
constexpr u32 L_S2 = L_R3;

constexpr u64 TAG_STEP = 0x9E3779B97F4A7C18ull;
constexpr u32 SPIN_CAP = 1u << 16;
constexpr u32 OOB = 0x80000000u;
DI constexpr u32 fprog(u32 r, u32 rt, u32 type, u32 j, u32 g) { return WS_FPROG + ((r * 4 + rt) * 4 + (j >> 1)) * 128 + (type * 4 + (j & 1) * 2 + g) * 8; }
DI constexpr u32 fkv(u32 j, u32 w) { return WS_FKV + (j * 8 + w) * 128; }

DI u32 f2u(float f) { return __builtin_bit_cast(u32, f); }
DI float u2f(u32 u) { return __builtin_bit_cast(float, u); }
template <int I> struct IC { static constexpr int v = I; };
template <int B, int E, class F> DI void sfor(F&& f) { if constexpr (B < E) { f(IC<B>{}); sfor<B + 1, E>(f); } }
DI u32 fresh_lane() { u32 l; asm volatile("v_mbcnt_lo_u32_b32 %0, -1, 0\n\tv_mbcnt_hi_u32_b32 %0, -1, %0" : "=v"(l)); return l; }
DI f32x16 mfma(u32x4 a, u32x4 b, f32x16 c) {
  return __builtin_amdgcn_mfma_f32_32x32x16_bf16(__builtin_bit_cast(bf16x8, a), __builtin_bit_cast(bf16x8, b), c, 0, 0, 0);
}
DI u32 pk2(float lo, float hi) { u32 r; asm("v_cvt_pk_bf16_f32 %0, %1, %2" : "=v"(r) : "v"(lo), "v"(hi)); return r; }
DI void nop2(u32x4& a) { asm volatile("s_nop 1" : "+v"(a)); }
DI u32x4 lds128(u32 a) { return *(LAS u32x4*)a; }
DI u32x2 ldstr(u32 a) { return __builtin_bit_cast(u32x2, __builtin_amdgcn_ds_read_tr16_b64_v4i16((LAS s16x4*)a)); }
DI u32x4 ldstr2(u32 a0, u32 a1) { u32x2 lo = ldstr(a0), hi = ldstr(a1); return u32x4{lo.x, lo.y, hi.x, hi.y}; }

template <int ID, u32 L, u32 K> DI void dma(u32 wl, u32 voff, rsrc_t rs, u32 sbase) {
  u32 t;
  asm volatile("s_add_u32 m0, %1, %5\n\ts_add_u32 %0, %4, %6\n\tbuffer_load_dwordx4 %2, %3, %0 offen lds ; op %7"
               : "=&s"(t) : "s"(wl), "v"(voff), "s"(rs), "s"(sbase), "i"(L), "i"(K), "i"(ID) : "memory", "scc");
}
template <int ID, int OFF> DI void ld128(u32x4& d, u32 voff, rsrc_t rs, u32 soff) {
  asm volatile("buffer_load_dwordx4 %0, %1, %2, %3 offen offset:%4 sc1 ; op %5" : "=v"(d) : "v"(voff), "s"(rs), "s"(soff), "i"(OFF), "i"(ID) : "memory");
}
template <int OFF> DI void rld128(u32x4& d, u32 voff, rsrc_t rs, u32 soff) {
  asm volatile("buffer_load_dwordx4 %0, %1, %2, %3 offen offset:%4 sc1" : "+v"(d) : "v"(voff), "s"(rs), "s"(soff), "i"(OFF) : "memory");
}
template <int ID> DI void ld64(u32x2& d, u32 voff, rsrc_t rs) {
  asm volatile("buffer_load_dwordx2 %0, %1, %2, 0 offen sc1 ; op %3" : "=v"(d) : "v"(voff), "s"(rs), "i"(ID) : "memory");
}
template <int ID, int OFF> DI void ld32(u32& d, u32 voff, rsrc_t rs, u32 soff) {
  asm volatile("buffer_load_dword %0, %1, %2, %3 offen offset:%4 sc1 ; op %5" : "=v"(d) : "v"(voff), "s"(rs), "s"(soff), "i"(OFF), "i"(ID) : "memory");
}
template <int ID, int OFF> DI void st128(u32x4 d, u32 voff, rsrc_t rs, u32 soff) {
  asm volatile("buffer_store_dwordx4 %0, %1, %2, %3 offen offset:%4 sc1 ; op %5\n\ts_nop 1" ::"v"(d), "v"(voff), "s"(rs), "s"(soff), "i"(OFF), "i"(ID) : "memory");
}
template <int ID> DI void st64(u32x2 d, u32 voff, rsrc_t rs) {
  asm volatile("buffer_store_dwordx2 %0, %1, %2, 0 offen sc1 ; op %3" ::"v"(d), "v"(voff), "s"(rs), "i"(ID) : "memory");
}
template <int ID> DI void st32(u32 d, u32 voff, rsrc_t rs, u32 soff) {
  asm volatile("buffer_store_dword %0, %1, %2, %3 offen sc1 ; op %4" ::"v"(d), "v"(voff), "s"(rs), "s"(soff), "i"(ID) : "memory");
}
template <int N, int NEED> DI void waitv() { asm volatile("s_waitcnt vmcnt(%0) ; need %1" ::"i"(N), "i"(NEED) : "memory"); }
template <int N, int NEED> DI void waitv(u32x2& a) { asm volatile("s_waitcnt vmcnt(%1) ; need %2" : "+v"(a) : "i"(N), "i"(NEED) : "memory"); }
template <int N, int NEED> DI void waitv(u32x4& a) { asm volatile("s_waitcnt vmcnt(%1) ; need %2" : "+v"(a) : "i"(N), "i"(NEED) : "memory"); }
DI void drain() { asm volatile("s_waitcnt vmcnt(0)" ::: "memory"); }
DI void tie(u32x4& a) { asm volatile("" : "+v"(a)); }
DI void tie(u32x2& a) { asm volatile("" : "+v"(a)); }
DI void tie(u32& a) { asm volatile("" : "+v"(a)); }
DI void tie(float& a) { asm volatile("" : "+v"(a)); }
DI void tie(f32x16& a) { asm volatile("" : "+v"(a)); }
template <int N, int NEED> DI void bar_v() { SB(); asm volatile("s_waitcnt vmcnt(%0) lgkmcnt(0) ; need %1\n\ts_barrier" ::"i"(N), "i"(NEED) : "memory"); SB(); }
template <int N, int NEED> DI void bar_w() { SB(); asm volatile("s_waitcnt vmcnt(%0) ; need %1\n\ts_barrier" ::"i"(N), "i"(NEED) : "memory"); SB(); }
DI void bar() { SB(); asm volatile("s_barrier" ::: "memory"); SB(); }
DI void bar_l() { SB(); asm volatile("s_waitcnt lgkmcnt(0)\n\ts_barrier" ::: "memory"); SB(); }

DI void poll(u64& f, const void* ws, u32 off) { asm volatile("s_load_dwordx2 %0, %1, %2 glc ; sop" : "=s"(f) : "s"(ws), "s"(off) : "memory"); }
DI void poll(u64x4& f, const void* ws, u32 off) { asm volatile("s_load_dwordx8 %0, %1, %2 glc ; sop" : "=s"(f) : "s"(ws), "s"(off) : "memory"); }
DI void poll(u64x8& f, const void* ws, u32 off) { asm volatile("s_load_dwordx16 %0, %1, %2 glc ; sop" : "=s"(f) : "s"(ws), "s"(off) : "memory"); }
template <class T> DI void polled(T& f) { asm volatile("s_waitcnt lgkmcnt(0)" : "+s"(f) :: "memory"); }
template <u32 BIT> DI bool match(u64 all, u64 any, u64 tag7) { return (EXP & 16) || (((all | (7u & ~BIT)) ^ tag7) | ((any | 7u) ^ tag7)) == 0; }
template <u32 BIT> DI bool ready(u64 f, u64 tag7) { return (EXP & 16) || (f | (7u & ~BIT)) == tag7; }
template <u32 BIT> DI bool ready(u64x4 f, u64 tag7) { return match<BIT>((f[0] & f[1]) & (f[2] & f[3]), (f[0] | f[1]) | (f[2] | f[3]), tag7); }
template <u32 BIT> DI bool ready(u64x8 f, u64 tag7) {
  return match<BIT>(((f[0] & f[1]) & (f[2] & f[3])) & ((f[4] & f[5]) & (f[6] & f[7])), ((f[0] | f[1]) | (f[2] | f[3])) | ((f[4] | f[5]) | (f[6] | f[7])), tag7);
}
template <u32 BITS> DI void publish(u64 tag7, u32 voff, rsrc_t rw) { st64<9>(u32x2{(u32)tag7 ^ (7u & ~BITS), (u32)(tag7 >> 32)}, voff, rw); }
template <class T, u32 BIT> DI void spin(const void* ws, u32 off, u64 tag7) {
  for (u32 n = 0; n < SPIN_CAP; ++n) {
    T f;
    poll(f, ws, off);
    polled(f);
    if (ready<BIT>(f, tag7)) break;
    __builtin_amdgcn_s_sleep(SPINSLEEP);
  }
}

#define STAMP(i) do {} while (0)
#define STAMP_ARG
#define STAMP_PASS

struct Ctx {
  u32 w, m, g;
  u32 r1, j, rot, type;
  u32 b;
  u64 tag7;
  const void* ws;
  rsrc_t rx, rW1, rW2, rw2, rwo, rw;
  u32 wl;
  u32 sB2, sBo;
  u32 sx, s1, s3, sT;
  u32 sin, sout;
  u32 vx, vW1, vW2, vB;
  u32 ax, ax3, aw0, aw1, aw2;
  u32 ah0, ah1, ah2;
  u32 vl, vpub;
  u32 cons, sfl;
};

template <int I> DI void dmaX0(const Ctx& c) {
  dma<200 + I, L_R0 + I * 8192, (448 * (I >> 2) + ((I >> 1) & 1) * 320 + (I & 1) * 64) * 1024>(c.wl, c.vB, c.rw2, c.sB2);
}
template <int I> DI void dmaX1(const Ctx& c) {
  dma<210 + I, L_R2 + I * 8192, (448 * (I / 3) + 128 + (I % 3) * 64) * 1024>(c.wl, c.vB, c.rw2, c.sB2);
}
template <int I> DI void dmaX2(const Ctx& c) { dma<220 + I, L_R1 + I * 8192, I * 65536>(c.wl, c.vB, c.rwo, c.sBo); }
template <int I> DI void lda(const Ctx& c, u32x4* a, u32 v) { ld128<300 + I, (I & 3) * 1024>(a[I], v, c.rw, c.sin + (I >> 2) * 4096); }

template <int TYPE> struct T1 {
  static constexpr u32 P1 = TYPE ? 3072 : 3584, P2 = TYPE ? 3584 : 3072;
  static constexpr u32 D12 = TYPE ? 1024 : 128;
  template <int POS> static DI void dmaW2(const Ctx& c) { dma<100 + POS, L_R0 + POS * 8192, D12>(c.wl, c.vW1, c.rW1, c.s1 + ((c.rot + POS) & 7) * (64 * P1)); }
  template <int POS> static DI void dmaW3(const Ctx& c) { dma<110 + POS, L_R2 + POS * 8192, 0>(c.wl, c.vW2, c.rW2, c.s3 + ((c.rot + POS) & 7) * (64 * P2)); }
  template <int IT, int I> static DI void later(const Ctx& c) {
    constexpr int POS = IT + PD;
    if constexpr (POS < 8) {
      const u32 kc = (c.rot + POS) & 7;
      if constexpr (I < 2) dma<10 * POS + 10 + I, ring(POS) + I * 8192, I * 65536>(c.wl, c.vx, c.rx, c.sx + kc * 128);
      else dma<10 * POS + 10 + I, ring(POS) + 16384, 0>(c.wl, c.vW1, c.rW1, c.s1 + kc * (64 * P1));
    } else if constexpr ((POS - 8) * 3 + I < 8) dmaW2<(POS - 8) * 3 + I>(c);
  }
};
template <int TYPE> DI f32x16 mm1(u32x4 wf, u32x4 xf, f32x16 acc) { acc = TYPE ? mfma(xf, wf, acc) : mfma(wf, xf, acc); tie(acc); return acc; }
DI f32x16 add_bias(u32 bias, u32 sel, f32x16 acc) { return mm1<0>(u32x4{bias, 0u, 0u, 0u}, u32x4{sel, 0u, 0u, 0u}, acc); }
DI u32x4 wfrag(u32 a) { return ldstr2(a, a + 512); }
template <int TYPE, int W> DI u32x4 wstep(const Ctx& c, u32 off, int s) {
  if (TYPE && (s & 2)) return wfrag((W == 0 ? c.ah0 : W == 1 ? c.ah1 : c.ah2) + off + (s - 2) * 2048);
  return wfrag((W == 0 ? c.aw0 : W == 1 ? c.aw1 : c.aw2) + off + s * 2048);
}

constexpr int NPRO = 2;
constexpr int NW2 = PD == 3 ? 8 : 6;
template <int TYPE, class F>
DI void pass1(const Ctx& c, u32x4 (&xf)[32], f32x16& acc, f32x16& aG, u32 gx, u32 ag, u32 agm, u32x4 (&fu)[4], F&& side STAMP_ARG) {
  typedef T1<TYPE> P;
  u32x4 wf[4], xg, gf;
  auto xaddr = [&](int pos, u32 x) { const u32 o = ring(pos) - L_RING; return o >= 3 * RING_SLOT ? (c.ax3 ^ x) + (o - 3 * RING_SLOT) : (c.ax ^ x) + o; };
  auto rd = [&](int pos, int t) {
    const u32 wo = ring(pos) + 16384;
    xf[4 * pos + t] = lds128(xaddr(pos, t << 5));
    wf[t] = wo < L_R1 ? wstep<TYPE, 0>(c, wo, t) : wo < L_R2 ? wstep<TYPE, 1>(c, wo - L_R1, t) : wstep<TYPE, 2>(c, wo - L_R2, t);
    SB();
  };
  rd(0, 0); rd(0, 1);
  P::template later<0, 0>(c); SB();
  sfor<0, 8>([&](auto I) {
    constexpr int pos = decltype(I)::v;
    rd(pos, 2);
    if constexpr (pos == 0) acc = mm1<TYPE>(wf[0], xf[0], f32x16{}); else acc = mm1<TYPE>(wf[0], xf[4 * pos], acc);
    SB();
    rd(pos, 3);
    if constexpr (TYPE == 0) {
      xg = lds128(xaddr(pos, gx)); gf = lds128(ag + ((((c.rot + pos) & 7) * 128) & agm)); SB();
    }
    P::template later<pos, 1>(c); SB();
    acc = mm1<TYPE>(wf[1], xf[4 * pos + 1], acc); SB();
    P::template later<pos, 2>(c); SB();
    constexpr int NT = (pos >= 1 && pos <= 5) + (PD == 3 && pos >= 2 && pos <= 6);
    if constexpr (pos < 7) bar_w<3 * (PD - 1) + NT + (pos + 1 < PD ? NPRO : 0), 10 * (pos + 1) + 12>(); else bar_w<NW2 - 3, 102>();
    STAMP(16 + pos);
    if constexpr (pos < 7) rd(pos + 1, 0); else { fu[0] = wfrag(c.aw0); SB(); }
    acc = mm1<TYPE>(wf[2], xf[4 * pos + 2], acc); SB();
    if constexpr (pos < 7) rd(pos + 1, 1); else { fu[1] = wfrag(c.aw0 + 2048); fu[2] = wstep<TYPE, 0>(c, 0, 2); SB(); }
    acc = mm1<TYPE>(wf[3], xf[4 * pos + 3], acc); SB();
    if constexpr (TYPE == 0) { if constexpr (pos == 0) aG = mm1<0>(gf, xg, f32x16{}); else aG = mm1<0>(gf, xg, aG); SB(); }
    side(I);
    if constexpr (pos < 7) { P::template later<pos + 1, 0>(c); SB(); }
  });
}

template <int NS, int SWAP, bool ZERO, class PRE, class RD, class POST>
DI void tile(u32x4 (&xf)[32], f32x16& acc, u32x4 (&fu)[4], PRE&& pre, RD&& rd, POST&& post) {
  sfor<0, NS>([&](auto I) {
    constexpr int s = decltype(I)::v;
    pre(I);
    rd(IC<s + 3>{}, fu[(s + 3) % 4]); SB();
    constexpr int xi = NS == 32 ? s : 4 * (s >> 1) + (s & 1);
    if constexpr (ZERO && s == 0) acc = mm1<SWAP>(fu[0], xf[0], f32x16{}); else acc = mm1<SWAP>(fu[s % 4], xf[xi], acc);
    SB();
    post(I); SB();
  });
}

template <int SL, int NSL, int V, class S> DI void mish4(const S& h, float (&e)[4], u32* out) {
  constexpr int cut[2][9] = {{0, 5, 8, 14, 18, 22, 26, 26, 26}, {0, 4, 6, 8, 12, 16, 18, 20, 26}};
  sfor<cut[NSL == 8][SL], cut[NSL == 8][SL + 1]>([&](auto I) {
    constexpr int o = decltype(I)::v, k = o & 3;
    if constexpr (o < 4) e[k] = __builtin_fmaf(h[V + k], 1.44269504f, -0.5f);
    else if constexpr (o < 8) e[k] = __builtin_amdgcn_exp2f(e[k]);
    else if constexpr (o < 12) e[k] += 0.70710678f;
    else if constexpr (o < 16) e[k] = __builtin_fmaf(e[k], e[k], 0.5f);
    else if constexpr (o < 20) e[k] = __builtin_amdgcn_rcpf(e[k]);
    else if constexpr (o < 24) e[k] = __builtin_fmaf(-h[V + k], e[k], h[V + k]);
    else out[k] = pk2(e[2 * k], e[2 * k + 1]);
    SB();
  });
}
template <int SL, int V> DI void phi4(const f32x16& t, float (&e)[8], u32* out) {
  constexpr int cut[7] = {0, 4, 6, 8, 12, 16, 22};
  sfor<cut[SL], cut[SL + 1]>([&](auto I) {
    constexpr int o = decltype(I)::v, k = o & 3;
    if constexpr (o < 4) e[k] = t[V + k] * 1.44269504f;
    else if constexpr (o < 8) e[k] = __builtin_amdgcn_fmed3f(__builtin_amdgcn_exp2f(e[k]), 0.f, 1.f);
    else if constexpr (o < 12) e[4 + k] = t[V + k] + 1e-6f;
    else if constexpr (o < 16) e[4 + k] = __builtin_fmaxf(e[4 + k], 1e-6f);
    else if constexpr (o < 20) e[k] += e[4 + k];
    else out[k] = pk2(e[2 * k], e[2 * k + 1]);
    SB();
  });
}

struct Early { u32 pf, b0, b1; };
DI u32 take_early(Early& e) {
  tie(e.pf); tie(e.b0); tie(e.b1);
  u32 bias = e.b0 | (e.b1 << 16);
  tie(bias); SB();
  return bias;
}
DI u32x4 vec4(const u32* p) { return u32x4{p[0], p[1], p[2], p[3]}; }

template <class F>
DI bool type_b(Ctx& c, Early& early, u32x4* a, u64x8& fl, F&& setup STAMP_ARG) {
  typedef T1<0> P;
  u32x4 xf[32], fu[4];
  f32x16 a0, a1, aQ, aG;
  const u32 lane = c.vl >> 4, jj = c.j & 1;
  {
    const u32 gq = jj * 2 + c.g;
    const bool row0 = (lane & 31) == 0;
    u32 ag = row0 ? L_GW + gq * 32 + (lane >> 5) * 16 : L_GW + 1024, agm = row0 ? ~0u : 0u;
    tie(ag); tie(agm);
    bar_v<3 * (PD - 1) + NPRO, 12>();
    STAMP(2);
    pass1<0>(c, xf, a0, aG, gq << 5, ag, agm, fu, [&](auto I) {
      if constexpr (decltype(I)::v == 2) {
        const u32 bias = take_early(early), one = lane >= 32 ? 0u : 0x3F80u;
        a0 = add_bias(bias, one, a0); a1 = add_bias(bias, one << 16, f32x16{}); SB();
      }
      setup(I, IC<0>{}); } STAMP_PASS);
  }
  STAMP(3);
  const u32 so = c.sout + jj * 4096 + c.g * 2048;
  float e[4];
  u32 pk[4];
  constexpr int S3 = PD == 3 ? 0 : 4;
  tile<32, 0, false>(xf, a1, fu,
    [&](auto I) { constexpr int s = decltype(I)::v;
      if constexpr (s == 9) bar_w<PD == 3 ? 6 : 4, 107>();
      if constexpr (s == 29) bar_w<PD == 3 ? 0 : 1, 117>(); },
    [&](auto I, u32x4& f) { constexpr int s = decltype(I)::v; f = wfrag(s < 32 ? c.aw0 + s * 2048 : c.aw2 + (s - 32) * 2048); },
    [&](auto I) { constexpr int s = decltype(I)::v;
      if constexpr (s >= 1) mish4<(s - 1) % 8, 8, 4 * ((s - 1) / 8)>(a0, e, pk + 2 * (((s - 1) / 8) & 1));
      if constexpr (s < S3 && !(s & 1)) P::dmaW2<6 + s / 2>(c);
      if constexpr (s >= S3 && s < S3 + 24 && !(s & 1)) { constexpr int i = (s - S3) / 2;
        if constexpr (PD == 3) { if constexpr (i < 4) dmaX2<i>(c); else P::dmaW3<i - 4>(c); }
        else { if constexpr (i < 3) dmaX2<i>(c); else if constexpr (i < 11) P::dmaW3<i - 3>(c); else dmaX2<3>(c); } }
      if constexpr (s == 2) tie(aG);
      if constexpr (s == 7) st32<6>(f2u(aG[0]), lane < 32 ? lane * 4 : OOB, c.rw, WS_GATE + (((c.r1 * 4 + c.m) * 8 + c.j) * 2 + c.g) * 128);
      if constexpr (s == 17) st128<1, 0>(vec4(pk), c.vl, c.rw, so);
      if constexpr (s == 29 || s == 31) dmaX0<(s - 29) / 2>(c); });
  STAMP(4);
  bool st0;
  u32 v0;
  u64x4 f4;
  u64x8 fk;
  tile<32, 0, true>(xf, aQ, fu,
    [&](auto I) { constexpr int s = decltype(I)::v;
      if constexpr (s == 13 || s == 25) bar();
      if constexpr (s == 11) { waitv<5, 2>(); publish<1>(c.tag7, c.vpub, c.rw); } },
    [&](auto I, u32x4& f) { constexpr int s = decltype(I)::v; if constexpr (s < 32) f = wfrag(c.aw2 + s * 2048); },
    [&](auto I) { constexpr int s = decltype(I)::v;
      if constexpr (s == 0) { mish4<7, 8, 12>(a0, e, pk + 2); st128<2, 1024>(vec4(pk), c.vl, c.rw, so); }
      else mish4<(s - 1) % 8, 8, 4 * ((s - 1) / 8)>(a1, e, pk + 2 * (((s - 1) / 8) & 1));
      if constexpr (s >= 2 && s <= 10 && !(s & 1)) dmaX0<1 + s / 2>(c);
      if constexpr (s == 13) dmaX0<7>(c);
      if constexpr (s == 15 || s == 19 || s == 21) dmaX1<s == 15 ? 0 : (s - 17) / 2>(c);
      if constexpr (s == 17) st128<3, 0>(vec4(pk), c.vl, c.rw, so + 8192);
      if constexpr (s == 22) poll(f4, c.ws, c.sfl);
      if constexpr (s == 25 || s == 27 || s == 29) dmaX1<3 + (s - 25) / 2>(c);
      if constexpr (s == 30) { polled(f4); st0 = ready<1>(f4, c.tag7); v0 = st0 ? c.vl : OOB; lda<0>(c, a, v0); } });
  mish4<7, 8, 12>(a1, e, pk + 2);
  st128<4, 1024>(vec4(pk), c.vl, c.rw, so + 8192);
  STAMP(5);
  u32 pq[8];
  {
    float e8[8];
    sfor<0, 24>([&](auto I) { constexpr int i = decltype(I)::v;
      if constexpr (i % 4 == 0 && i < 20) { lda<1 + i / 4>(c, a, v0); SB(); }
      if constexpr (i == 12) { poll(fk, c.ws, fkv(c.j, c.w)); SB(); }
      phi4<i % 6, 4 * (i / 6)>(aQ, e8, pq + 2 * (i / 6)); });
#pragma unroll
    for (int i = 0; i < 8; ++i) tie(pq[i]);
  }
  waitv<5, 4>();
  publish<3>(c.tag7, c.vpub, c.rw);
  polled(fk);
  STAMP(31);
  {
    const u64 t2 = ((c.tag7 & 0xFFFFFFFFull) << 32) | (c.tag7 & 0xFFFFFFFFull);
    if (__builtin_expect(!ready<7>(fk, t2), 0)) spin<u64x8, 7>(c.ws, fkv(c.j, c.w), t2);
  }
  STAMP(6);
  const u32 h = lane >> 5, kvrec = (c.j * 2 + c.g) * 16;
  const bool zw = c.m == 2 + c.g;
  u32x4 pr[8], pz[2];
  const u32 vp = h * 2048 + c.m * 512 + (lane & 31) * 16;
  { const u32 vz = zw ? c.vl : OOB; ld128<400, 0>(pz[0], vz, c.rw, WS_ZP + kvrec * 128); ld128<401, 1024>(pz[1], vz, c.rw, WS_ZP + kvrec * 128); }
  sfor<0, 8>([&](auto I) { constexpr int i = decltype(I)::v; ld128<402 + i, 0>(pr[i], vp, c.rw, WS_KVP + (kvrec + 2 * i) * 2048); });
  lda<6>(c, a, v0); lda<7>(c, a, v0);
  bar_w<10, 401>();
  tie(pz[0]); tie(pz[1]);
  const u32 s2 = L_S2 + c.g * 1088;
  if (zw) {
    f32x4 u = __builtin_bit_cast(f32x4, pz[0]) + __builtin_bit_cast(f32x4, pz[1]);
#pragma unroll
    for (int i = 0; i < 4; ++i) {
      u[i] += u2f(__builtin_amdgcn_update_dpp(0u, f2u(u[i]), 0x128, 0xF, 0xF, false));
      const auto r = __builtin_amdgcn_permlane16_swap(f2u(u[i]), f2u(u[i]), false, false);
      u[i] = u2f(r[0]) + u2f(r[1]);
      const auto q = __builtin_amdgcn_permlane32_swap(f2u(u[i]), f2u(u[i]), false, false);
      u[i] = u2f(q[0]) + u2f(q[1]);
    }
    *(LAS u32x2*)(s2 + 1024 + (lane & 7) * 8) = u32x2{pk2(u[0], u[1]), pk2(u[2], u[3])};
  }
  const bool hi = (lane & 16) != 0;
  const u32 aA0 = hi ? s2 + 1024 + h * 16 : s2 + c.vl, aA1 = hi ? s2 + c.vl : s2 + 1024 + 32 + h * 16;
  const u32 aS = s2 + ((lane & 31) + 32 * (c.m & 1)) * 16 + (c.m >> 1) * 8 + h * 4;
  SB();
  waitv<2, 409>();
#pragma unroll
  for (int i = 0; i < 8; ++i) tie(pr[i]);
  STAMP(24);
  {
    f32x4 t = ((__builtin_bit_cast(f32x4, pr[0]) + __builtin_bit_cast(f32x4, pr[1])) + (__builtin_bit_cast(f32x4, pr[2]) + __builtin_bit_cast(f32x4, pr[3]))) +
              ((__builtin_bit_cast(f32x4, pr[4]) + __builtin_bit_cast(f32x4, pr[5])) + (__builtin_bit_cast(f32x4, pr[6]) + __builtin_bit_cast(f32x4, pr[7])));
    const auto r0 = __builtin_amdgcn_permlane32_swap(f2u(t[0]), f2u(t[2]), false, false);
    const auto r1 = __builtin_amdgcn_permlane32_swap(f2u(t[1]), f2u(t[3]), false, false);
    *(LAS u32*)aS = pk2(u2f(r0[0]) + u2f(r0[1]), u2f(r1[0]) + u2f(r1[1]));
  }
  bar_l();
  STAMP(25);
  u32x4 A0 = lds128(aA0), A1 = lds128(aA1);
  tie(A0); tie(A1);
  poll(fl, c.ws, c.sfl);
  f32x16 Y0 = mm1<0>(A0, vec4(pq), f32x16{});
  f32x16 Y1 = mm1<0>(A1, vec4(pq + 4), f32x16{});
  const float i0 = __builtin_amdgcn_rcpf(Y0[8] + 1e-6f), i1 = __builtin_amdgcn_rcpf(Y1[0] + 1e-6f);
  u32x4 o0, o1;
#pragma unroll
  for (int i = 0; i < 4; ++i) o0[i] = pk2(Y0[2 * i] * i0, Y0[2 * i + 1] * i0);
#pragma unroll
  for (int i = 0; i < 4; ++i) o1[i] = pk2(Y1[8 + 2 * i] * i1, Y1[9 + 2 * i] * i1);
  SB();
  st128<410, 0>(o0, c.vl, c.rw, so + 20480);
  st128<411, 1024>(o1, c.vl, c.rw, so + 20480);
  SB();
  STAMP(26);
  waitv<2, 307>();
  polled(fl);
#pragma unroll
  for (int i = 0; i < 8; ++i) tie(a[i]);
  return st0;
}

template <class F>
DI bool type_a(Ctx& c, Early& early, u32x4* a, u64x8& fl, F&& setup STAMP_ARG) {
  typedef T1<1> P;
  u32x4 xf[32], fu[4];
  f32x16 aK, aV;
  u32 bias;
  c.ah0 = c.aw0 + 4096 - 4096 * c.g; c.ah1 = c.ah0 + L_R1; c.ah2 = c.ah0 + L_R2;
  c.aw0 += 4096 * c.g; c.aw1 += 4096 * c.g; c.aw2 += 4096 * c.g; c.ax ^= c.g << 6; c.ax3 ^= c.g << 6;
  tie(c.ah0); tie(c.ah1); tie(c.ah2); tie(c.aw0); tie(c.aw1); tie(c.aw2); tie(c.ax); tie(c.ax3);
  bar_v<3 * (PD - 1) + NPRO, 12>();
  STAMP(2);
  pass1<1>(c, xf, aK, aV, 0u, 0u, 0u, fu, [&](auto I) { if constexpr (decltype(I)::v == 2) bias = take_early(early); setup(I, IC<1>{}); } STAMP_PASS);
  const u32 lane = c.vl >> 4, h = lane >> 5, jj = c.j & 1;
  const u32 vT = (lane >> 2) * 3584 + (lane & 3) * 16;
  u32 at = L_R1 + c.g * 2048 + (8 * h + ((lane & 15) >> 2)) * 64 + ((lane >> 4) & 1) * 32 + (lane & 3) * 8;
  u32 him = (lane & 16) ? 0xFFFFFFFFu : 0u;
  tie(at); tie(him);
  auto sel = [&](float p, float q) { return u2f((f2u(p) & him) | (f2u(q) & ~him)); };
  auto tfrag = [&](int s) { const u32 o = at + (s >> 1) * 4096 + (s & 1) * 1024; return ldstr2(o, o + 256); };
  const u32 sb = L_S1 + c.w * 2176;
  u32 rb = L_S1 + c.g * 4 * 2176 + (lane < 32 ? c.m * 512 + c.vl : 2048 + (lane & 7) * 16);
  const u32 kvrec = (c.j * 2 + c.g) * 16 + c.r1;
  u32 vk = lane < 32 ? WS_KVP + kvrec * 2048 + c.m * 512 + c.vl : (lane < 40 && c.m == 2 + c.g) ? WS_ZP + kvrec * 128 + (lane & 7) * 16 : OOB;
  STAMP(3);
  u32 pkk[8];
  float e8[8];
  f32x16 Z;
  const u32x4 ones = {0x3F803F80u, 0x3F803F80u, 0x3F803F80u, 0x3F803F80u};
  tile<32, 1, true>(xf, aV, fu,
    [&](auto I) { constexpr int s = decltype(I)::v;
      if constexpr (s == 9) bar_w<PD == 3 ? 3 : 2, 107>();
      if constexpr (s == 29) bar_w<PD == 3 ? 8 : 6, 123>(); },
    [&](auto I, u32x4& f) { constexpr int s = decltype(I)::v;
      if constexpr (s < 32) f = wstep<1, 0>(c, 0, s); else f = tfrag(s - 32); },
    [&](auto I) { constexpr int s = decltype(I)::v;
      if constexpr (s >= 1 && s <= 24) phi4<(s - 1) % 6, 4 * ((s - 1) / 6)>(aK, e8, pkk + 2 * ((s - 1) / 6));
      if constexpr (s == 25) Z = mm1<0>(vec4(pkk), ones, f32x16{});
      if constexpr (s == 26) Z = mm1<0>(vec4(pkk + 4), ones, Z);
      if constexpr (s == 27) tie(rb);
      if constexpr (s == 28) tie(vk);
      if constexpr (s == 29 || s == 30) { constexpr int k = 4 * (s - 29);
        const u32 zoff = (lane & 15) == 0 ? sb + 2048 + (((lane >> 4) & 1) * 2 + h) * 32 : L_S1 + 8 * 2176;
        *(LAS f32x4*)(zoff + 4 * k) = f32x4{sel(Z[8 + k], Z[k]), sel(Z[9 + k], Z[1 + k]), sel(Z[10 + k], Z[2 + k]), sel(Z[11 + k], Z[3 + k])}; }
      SB();
      constexpr int u = s - (PD == 3 ? 0 : 4);
      if constexpr (u < 0 && !(s & 1)) P::dmaW2<6 + s / 2>(c);
      if constexpr (u == 0 || u == 3 || u == 7 || u == 10) { constexpr int i = u / 3;
        dma<120 + i, L_R1 + i * 8192, 0>(c.wl, vT, c.rW2, c.sT + ((c.rot + 2 * i + c.g) & 7) * (64 * 3584)); }
      if constexpr (u >= 13 && u <= 27 && (u & 1)) P::dmaW3<(u - 13) / 2>(c);
      if constexpr (u == 29 || u == 31) dmaX0<3 + (u - 29) / 2>(c); });
  STAMP(4);
  f32x16 aT, R;
  u32 v[8];
  f32x4 t4[4];
  auto t_pre = [&](auto I) { constexpr int s = decltype(I)::v;
    if constexpr (s == 5) bar_v<PD == 3 ? 6 : 5, 115>();
    if constexpr (s == 11) bar();
    if constexpr (s == 15) { waitv<PD == 3 ? 3 : 4, 420>(); st32<9>((u32)c.tag7, lane == 0 ? fkv(c.j, c.w) + c.r1 * 4 : OOB, c.rw, 0); } };
  auto t_rd = [&](auto I, u32x4& f) { constexpr int s = decltype(I)::v;
    if constexpr (s < 16) f = tfrag(s); else f = wstep<1, 2>(c, 0, s - 16); };
  auto t_post = [&](auto I) { constexpr int s = decltype(I)::v;
    if constexpr (s == 1) {
      asm volatile("s_nop 1" : "+v"(aV));
#pragma unroll
      for (int i = 0; i < 8; ++i) { v[i] = pk2(aV[2 * i], aV[2 * i + 1]); tie(v[i]); }
      SB();
      R = mm1<0>(vec4(pkk), vec4(v), f32x16{});
    }
    if constexpr (s == 2) R = mm1<0>(vec4(pkk + 4), vec4(v + 4), R);
    if constexpr (s == 4) {
#pragma unroll
      for (int k = 0; k < 8; k += 4)
        *(LAS f32x4*)(sb + k * 256 + c.vl) = f32x4{sel(R[8 + k], R[k]), sel(R[9 + k], R[1 + k]), sel(R[10 + k], R[2 + k]), sel(R[11 + k], R[3 + k])};
    }
    if constexpr (s == 5) {
#pragma unroll
      for (int i = 0; i < 4; ++i) t4[i] = *(LAS f32x4*)(rb + i * 2176);
    }
    if constexpr (s == 6) {
      u32x4 o;
#pragma unroll
      for (int i = 0; i < 4; ++i) { float u = (t4[0][i] + t4[1][i]) + (t4[2][i] + t4[3][i]); tie(u); o[i] = f2u(u); }
      st128<420, 0>(o, vk, c.rw, 0);
    }
    SB();
    constexpr int X = PD == 3 ? 5 : 3;
    if constexpr ((s == 0 || s == 2 || s == 3) && X + (s + 1) / 2 < 7) dmaX0<X + (s + 1) / 2>(c);
    if constexpr (s == 7 && PD == 2) dmaX0<6>(c);
    if constexpr (s == 9) dmaX0<7>(c);
    if constexpr (s == 11 || s == 13 || s == 15) dmaX0<(s - 11) / 2>(c); };
  tile<16, 0, true>(xf, aT, fu, t_pre, t_rd, t_post);
  STAMP(24);
  const u32 one = lane >= 32 ? 0u : 0x3F80u;
  aT = add_bias(bias, c.g ? 0u : one << 16, aT); SB();
  f32x16 aU = add_bias(bias, one, f32x16{}); SB();
  u32 gm = c.g ? 0xFFFFFFFFu : 0u;
  auto gsel = [&](float p, float q) { return u2f((f2u(p) & gm) | (f2u(q) & ~gm)); };
  const u32 sc = L_R1 + c.w * 2048 + c.vl, sr = sc ^ 8192;
  const u32 so = c.sout + 28672 + jj * 4096 + c.g * 2048;
  float hT[8], e[4];
  f32x4 p[2];
  u32 pk[4];
  bool st0;
  u32 vs;
  u64x4 f4;
  tile<32, 0, false>(xf, aU, fu,
    [&](auto I) { constexpr int s = decltype(I)::v;
      if constexpr (s == 3) bar_l();
      if constexpr (s == 13 || s == 25) bar();
      if constexpr (s == 30) { waitv<5, 1>(); publish<3>(c.tag7, c.vpub, c.rw); } },
    [&](auto I, u32x4& f) { constexpr int s = decltype(I)::v; if constexpr (s < 32) f = wstep<1, 2>(c, 0, s); },
    [&](auto I) { constexpr int s = decltype(I)::v;
      if constexpr (s == 1 || s == 2) { constexpr int k = 4 * (s - 1);
        *(LAS f32x4*)(sc + (s - 1) * 1024) = f32x4{gsel(aT[k], aT[8 + k]), gsel(aT[k + 1], aT[9 + k]), gsel(aT[k + 2], aT[10 + k]), gsel(aT[k + 3], aT[11 + k])}; }
      if constexpr (s == 3) { p[0] = *(LAS f32x4*)sr; p[1] = *(LAS f32x4*)(sr + 1024); }
      if constexpr (s == 3 || s == 4) {
#pragma unroll
        for (int i = 4 * (s - 3); i < 4 * (s - 3) + 4; ++i) { hT[i] = gsel(aT[8 + i], aT[i]); tie(hT[i]); }
      }
      if constexpr (s == 5 || s == 6) {
#pragma unroll
        for (int i = 0; i < 4; ++i) { hT[4 * (s - 5) + i] += p[s - 5][i]; tie(hT[4 * (s - 5) + i]); }
      }
      SB();
      if constexpr (s >= 7 && s < 19) mish4<(s - 7) % 6, 6, 4 * ((s - 7) / 6)>(hT, e, pk + 2 * ((s - 7) / 6));
      if constexpr (s == 3 || s == 5) dmaX2<2 + (s - 3) / 2>(c);
      if constexpr (s == 4) poll(f4, c.ws, c.sfl);
      if constexpr (s == 10) { polled(f4); st0 = ready<1>(f4, c.tag7); vs = st0 ? c.vl : OOB; }
      if constexpr (s == 10 || s == 11) lda<s - 10>(c, a, vs);
      if constexpr (s == 13 || s == 15) dmaX2<(s - 13) / 2>(c);
      if constexpr (s == 16 || s == 18) lda<2 + (s - 16) / 2>(c, a, vs);
      if constexpr (s == 19) st128<1, 0>(vec4(pk), c.vl, c.rw, c.sout + 16384 + jj * 2048 + c.g * 1024);
      if constexpr (s == 21 || s == 22 || s == 24 || s == 26) lda<s == 21 ? 4 : 5 + (s - 22) / 2>(c, a, vs);
      if constexpr (s == 28 || s == 30 || s == 31) dmaX1<s == 28 ? 0 : s - 29>(c); });
  STAMP(25);
  sfor<0, 32>([&](auto I) { constexpr int i = decltype(I)::v;
    if constexpr (i % 4 == 2 && i < 12) { dmaX1<3 + i / 4>(c); SB(); }
    if constexpr (i == 16) { st128<2, 0>(vec4(v), c.vl, c.rw, so); SB(); }
    if constexpr (i == 18) { poll(fl, c.ws, c.sfl); SB(); }
    mish4<i % 8, 8, 4 * (i / 8)>(aU, e, v + 2 * (i / 8)); });
  st128<3, 1024>(vec4(v + 4), c.vl, c.rw, so); SB();
  STAMP(26);
  polled(fl);
  bar_w<2, 215>();
#pragma unroll
  for (int i = 0; i < 8; ++i) tie(a[i]);
  return st0;
}

DI constexpr u32 bblock(int s) {
  if (s < 16) return ((s >> 2) & 1) * 16384 + (s >> 3) * 8192 + (s & 3) * 2048;
  if (s < 20) return 8192 + (s - 16) * 2048;
  if (s < 28) return (s - 20) * 2048;
  return (s < 32 ? 0 : 16384) + (s & 3) * 2048;
}

struct P2 {
  u32 ab[3][2];
  u32x4 f0[3], f1[3];
  f32x16 cB0, cB1, cA0, cA1;
  DI u32x4 rd(int s, int n) const {
    const int sec = s < 16 ? 0 : (s >= 20 && s < 28) ? 1 : 2;
    return ldstr2(ab[sec][n] + bblock(s), ab[sec][n] + bblock(s) + 1024);
  }
  template <int B, int E, class F, class G> DI void run(u32x4 (&a)[36], F&& pre, G&& post) {
    sfor<B, E>([&](auto I) {
      constexpr int S = decltype(I)::v;
      constexpr bool PA = S >= 20 && S < 28, Z = S == 0 || S == 20;
      pre(I); SB();
      if constexpr (PA) cA0 = mm1<0>(f0[S % 3], a[S], Z ? f32x16{} : cA0); else cB0 = mm1<0>(f0[S % 3], a[S], Z ? f32x16{} : cB0);
      SB();
      if constexpr (S + 2 < 36) { f0[(S + 2) % 3] = rd(S + 2, 0); SB(); }
      post(I, IC<0>{}); SB();
      if constexpr (PA) cA1 = mm1<0>(f1[S % 3], a[S], Z ? f32x16{} : cA1); else cB1 = mm1<0>(f1[S % 3], a[S], Z ? f32x16{} : cB1);
      SB();
      if constexpr (S + 2 < 36) { f1[(S + 2) % 3] = rd(S + 2, 1); SB(); }
      post(I, IC<1>{}); SB();
    });
  }
};

}

__global__ __launch_bounds__(512, 2) void gated_mix_kernel(const void* x, const void* wqkv, const void* wo, const void* w1, const void* b1,
                                                           const void* w2, const void* gw, void* out, void* ws) {
  __shared__ __attribute__((aligned(1024))) char smem[L_END];
  STAMP(0);
  const u32 b = blockIdx.x, xc = b & 7, slot = b >> 3;
  Early early;
  {
    u64 pc; asm volatile("s_getpc_b64 %0" : "=s"(pc));
    const u32 ln = (threadIdx.x >> 6) * 32 + slot;
    asm volatile("global_load_dword %0, %1, %2" : "=v"(early.pf) : "v"((ln < CODE_LINES ? ln : 0) * 128), "s"(pc & ~127ull) : "memory");
  }
  Ctx c;
  const u32 lane = threadIdx.x & 63;
  c.w = __builtin_amdgcn_readfirstlane(threadIdx.x >> 6); c.m = c.w & 3; c.g = c.w >> 2;
  c.type = slot >> 4;
  c.b = b;
  c.r1 = (xc >> 2) * 8 + (slot & 7); c.j = 2 * (xc & 3) + ((slot >> 3) & 1);
  c.rot = (ROTA * (slot & 7) + ROTB * (slot >> 3) + ROTC * (xc & 3) + ROTD * (xc >> 2)) & 7;
  const u32 lbase = (u32)(uintptr_t)(LAS char*)smem;
  c.wl = lbase + c.w * 1024;
  const u32 p1 = 3584 - 512 * c.type, p2 = 3072 + 512 * c.type, nt = 1 - c.type, odd = c.j & 1;
  const u32 cu1 = (448 - 320 * c.type) * c.j + 1024 * c.type + 192 * (odd & nt);
  c.sx = (c.r1 * 128 + 8 * c.w) * 1024;
  c.s1 = 8 * c.w * p1 + cu1;
  const u32 lrow = lane >> 3, lpos = lane & 7;
  c.vx = lrow * 1024 + ((lpos ^ ((4 * (c.w & 1) + (lane >> 4)) & 7)) * 16);
  const u32 wsw = (lpos ^ (((lane >> 4) & 1) << 2)) * 16;
  c.vW1 = lrow * p1 + wsw;
  c.vl = lane * 16;
  const u32 k1 = (c.rot + 1) & 7, k2 = (c.rot + 2) & 7;
  u32 sx0 = c.sx + c.rot * 128, sw0 = c.s1 + c.rot * 64 * p1, sx1 = c.sx + k1 * 128, sw1 = c.s1 + k1 * 64 * p1, nW1 = 512 * p1, wodd = c.wl & 1024, vg = c.vl + wodd;
  asm volatile("" : "+s"(sx0), "+s"(sw0), "+s"(sx1), "+s"(sw1), "+s"(nW1), "+s"(wodd), "+s"(c.wl));
  tie(c.vx); tie(c.vW1); tie(vg);
  SB();
  asm volatile("" : "+s"(x), "+s"(wqkv), "+s"(wo), "+s"(w1), "+s"(b1), "+s"(w2), "+s"(gw), "+s"(out), "+s"(ws));
  STAMP(30);
  c.rx = __builtin_amdgcn_make_buffer_rsrc((void*)x, 0, (EXP & 64) ? 0 : 2048 * 1024, 0x00020000);
  dma<10, ring(0), 0>(c.wl, c.vx, c.rx, sx0);
  dma<11, ring(0) + 8192, 65536>(c.wl, c.vx, c.rx, sx0);
  SB();
  c.rW1 = __builtin_amdgcn_make_buffer_rsrc((void*)(c.type ? wqkv : w1), 0, (EXP & 64) ? 0 : nW1, 0x00020000);
  dma<13, ring(0) + 16384, 0>(c.wl, c.vW1, c.rW1, sw0);
  SB();
  dma<12, L_GW, 0>(wodd, vg, __builtin_amdgcn_make_buffer_rsrc((void*)gw, 0, 1024, 0x00020000), 0u);
  dma<20, ring(1), 0>(c.wl, c.vx, c.rx, sx1);
  dma<21, ring(1) + 8192, 65536>(c.wl, c.vx, c.rx, sx1);
  dma<22, ring(1) + 16384, 0>(c.wl, c.vW1, c.rW1, sw1);
  SB();
  u64 ep;
  asm volatile("s_load_dwordx2 %0, %1, 0x0 ; sop" : "=s"(ep) : "s"(ws) : "memory");
  if constexpr (PD == 3) {
    dma<30, ring(2), 0>(c.wl, c.vx, c.rx, c.sx + k2 * 128);
    dma<31, ring(2) + 8192, 65536>(c.wl, c.vx, c.rx, c.sx + k2 * 128);
    dma<32, ring(2) + 16384, 0>(c.wl, c.vW1, c.rW1, c.s1 + k2 * 64 * p1);
    SB();
  }
  STAMP(1);
  {
    const u32 cuU = 448 * c.j + 256 - 192 * odd, cuT = 448 * c.j + 384 - 384 * odd;
    asm volatile("buffer_load_ushort %0, %2, %3, %4 offen ; op 15\n\tbuffer_load_ushort %1, %2, %3, %5 offen ; op 16"
                 : "=&v"(early.b0), "=&v"(early.b1)
                 : "v"((lane & 31) * 2), "s"(__builtin_amdgcn_make_buffer_rsrc((void*)b1, 0, 3584, 0x00020000)),
                   "s"(c.type * cuU + nt * cu1 + 64 * c.g), "s"(c.type * cuT + nt * (cu1 + 128 + 64 * c.g)) : "memory");
    c.s3 = 8 * c.w * p2 + c.type * cuU + nt * 128 * c.j; c.sT = 16 * c.m * 3584 + cuT;
  }
  SB();
  c.rW2 = __builtin_amdgcn_make_buffer_rsrc((void*)(c.type ? w1 : wqkv), 0, (EXP & 8) ? 0 : 512 * p2, 0x00020000);
  c.rw2 = __builtin_amdgcn_make_buffer_rsrc((void*)w2, 0, (EXP & 1) ? 0 : 1792 * 1024, 0x00020000);
  c.rwo = __builtin_amdgcn_make_buffer_rsrc((void*)wo, 0, (EXP & 1) ? 0 : 512 * 1024, 0x00020000);
  c.rw = __builtin_amdgcn_make_buffer_rsrc(ws, 0, (EXP & 32) ? 0 : WS_END, 0x00020000);
  {
    const u32 h = lane >> 5, row = 32 * c.m + (lane & 31);
    c.ax = lbase + L_RING + row * 128 + ((h ^ ((row >> 1) & 7)) * 16);
    c.ax3 = c.ax + 3 * RING_SLOT;
    const u32 q = (lane & 15) >> 2, pp = lane & 3, blk = (lane >> 4) & 1;
    c.aw0 = lbase + (8 * h + q) * 128 + (((4 * c.g + 2 * blk + (pp >> 1)) ^ ((q >> 1) << 2)) * 16) + 8 * (pp & 1);
    c.aw1 = c.aw0 + L_R1; c.aw2 = c.aw0 + L_R2;
    tie(c.aw0); tie(c.aw1); tie(c.aw2); tie(c.ax); tie(c.ax3); tie(c.vl);
  }
  u32 r2 = (xc >> 1) * 4 + (slot >> 3), sy;
  {
    const u32 ct = slot & 7, kh = xc & 1, kq = c.m >> 1, rt = 2 * (kh ^ c.g) + (c.m & 1);
    c.cons = (r2 * 4 + rt) * 4 + 2 * kh + kq;
    c.sfl = WS_FPROG + c.cons * 128;
    c.sin = WS_HND + c.cons * 36864;
    c.sout = WS_HND + ((c.r1 * 4 + c.m) * 4 + (c.j >> 1)) * 36864;
    c.sBo = (256 * kh + 8 * c.w) * 1024 + ct * 128; c.sB2 = c.sBo + 640 * 1024 * kh;
    sy = (r2 * 128 + rt * 32) * 1024 + ct * 128 + kq * 64;
    asm volatile("" : "+s"(c.cons), "+s"(c.sfl), "+s"(c.sin), "+s"(c.sout), "+s"(c.sBo), "+s"(c.sB2), "+s"(sy), "+s"(c.s3), "+s"(c.sT), "+s"(r2));
  }
  polled(ep);
  c.tag7 = (ep + TAG_STEP) | 7;
  c.ws = ws;
  P2 p;
  u32 pw = 0;
  auto setup = [&](auto I, auto TY) {
    constexpr int pos = decltype(I)::v, TYPE = decltype(TY)::v;
    constexpr u32 P1 = T1<TYPE>::P1, P2 = T1<TYPE>::P2;
    u32 ln = c.vl, w = c.w, r1 = c.r1;
    tie(ln); asm volatile("" : "+s"(w), "+s"(r1));
    ln >>= 4;
    if constexpr (pos == 1) {
      const u32 ws16 = ((ln & 7) ^ (((ln >> 4) & 1) << 2)) * 16;
      c.vW2 = (ln >> 3) * P2 + ws16; c.vB = (ln >> 3) * 1024 + ws16;
      tie(c.vW2); tie(c.vB);
    }
    if constexpr (pos == 5) { c.vpub = ln == 0 ? fprog(r1, w & 3, TYPE, c.j, w >> 2) : OOB; tie(c.vpub); }
    if constexpr (pos == 6) {
      const u32 h = ln >> 5, q = (ln & 15) >> 2, pp = ln & 3, blk = (ln >> 4) & 1, kq = (w >> 1) & 1;
      const u32 a0 = lbase + (4 * h + q) * 128 + (((4 * kq + 2 * blk + (pp >> 1)) ^ ((q >> 1) << 2)) * 16) + 8 * (pp & 1);
      p.ab[0][0] = a0 + L_R0 + kq * 32768; p.ab[1][0] = a0 + L_R1 + kq * 16384; p.ab[2][0] = a0 + L_R2 + kq * 24576;
#pragma unroll
      for (int i = 0; i < 3; ++i) { p.ab[i][1] = p.ab[i][0] ^ 64; tie(p.ab[i][0]); tie(p.ab[i][1]); }
    }
#define TOUCH(rs, n, v, so) asm volatile("buffer_load_dword %0, %1, %2, %3 offen ; op 17" : "+v"(pw) : "v"(ln < (PFMODE ? (n) : 0) ? (v) : OOB), "s"(rs), "s"(so) : "memory")
    if constexpr (pos == 0) TOUCH(c.rW1, 4, ln * P1, c.s1 + (r1 * 32 - w * 4) * P1 + T1<TYPE>::D12);
    if constexpr (pos == 1) TOUCH(c.rW2, 4 + 4 * TYPE, (ln & 3) * P2 + (ln < 4 ? c.s3 - 8 * w * P2 : c.sT - 16 * (w & 3) * P2), (r1 * 32 + w * 4) * P2);
    if constexpr (pos == 2) TOUCH(c.rw2, 7, ln * 1024, c.sB2 + (r2 * 56 - w) * 1024);
    if constexpr (pos == 3) TOUCH(c.rwo, 2, ln * 1024, c.sBo + (r2 * 16 - w * 6) * 1024);
    if constexpr (pos == 4) TOUCH(c.rw, 1, 0, WS_FPROG + (w * 256 + b) * 128);
    if constexpr (pos == 7) tie(pw);
    SB();
  };

  u32x4 a[36];
  u64x8 fl;
  bool st0;
  if (c.type == 0) st0 = type_b(c, early, a, fl, setup STAMP_PASS);
  else st0 = type_a(c, early, a, fl, setup STAMP_PASS);
  STAMP(7);

  const u32 sg = WS_GATE + c.cons * 512, spo = WS_PART + (b * 4 + c.m) * 4096, sfp = WS_FP + ((b ^ 1 ^ c.g) * 4 + c.m) * 128;
  p.f0[0] = p.rd(0, 0); p.f1[0] = p.rd(0, 1); p.f0[1] = p.rd(1, 0); p.f1[1] = p.rd(1, 1); SB();
  if (__builtin_expect(!st0, 0)) {
    STAMP(27);
    spin<u64x4, 1>(ws, c.sfl, c.tag7);
#pragma unroll
    for (int i = 0; i < 2; ++i) { rld128<0>(a[4 * i], c.vl, c.rw, c.sin + i * 4096); rld128<1024>(a[4 * i + 1], c.vl, c.rw, c.sin + i * 4096);
                                  rld128<2048>(a[4 * i + 2], c.vl, c.rw, c.sin + i * 4096); rld128<3072>(a[4 * i + 3], c.vl, c.rw, c.sin + i * 4096); }
    drain();
#pragma unroll
    for (int i = 0; i < 8; ++i) tie(a[i]);
    STAMP(29);
  }
  const bool mid = ready<2>(fl, c.tag7);
  STAMP(8);
  u32 gl0, gl1;
  float gate;
  auto none = [](auto) {};
  auto none2 = [](auto, auto) {};
  auto mk_gate = [&]() {
    tie(gl0); tie(gl1);
    float l = u2f(gl0) + u2f(gl1);
    const auto s = __builtin_amdgcn_permlane32_swap(f2u(l), f2u(l), false, false);
    gate = __builtin_amdgcn_rcpf(1.f + __builtin_amdgcn_exp2f(-1.44269504f * (u2f(s[0]) + u2f(s[1]))));
    tie(gate);
  };
  auto ld_gate = [&]() { ld32<340, 0>(gl0, c.vl >> 2, c.rw, sg); ld32<341, 256>(gl1, c.vl >> 2, c.rw, sg); };
  auto scale_a = [&](auto I, auto J) { constexpr int S = decltype(I)::v, H = decltype(J)::v;
    if constexpr (S >= 30 && S < 34) {
#pragma unroll
      for (int i = 4 * (S - 30); i < 4 * (S - 30) + 4; ++i) { float u = (H ? p.cA1[i] : p.cA0[i]) * gate; tie(u); if (H) p.cA1[i] = u; else p.cA0[i] = u; }
    } };
  float r0[16], r1[16];
  auto mix = [&]() {
    const float ng = 1.f - gate;
#pragma unroll
    for (int i = 0; i < 16; ++i) { r0[i] = __builtin_fmaf(ng, p.cB0[i], p.cA0[i]); tie(r0[i]); }
#pragma unroll
    for (int i = 0; i < 16; ++i) { r1[i] = __builtin_fmaf(ng, p.cB1[i], p.cA1[i]); tie(r1[i]); }
  };
  if (c.g) {
    __builtin_amdgcn_s_setprio(1);
    if (__builtin_expect(!mid, 0)) { STAMP(28); spin<u64x8, 2>(ws, c.sfl, c.tag7); }
    ld_gate();
    p.run<0, 8>(a, none, [&](auto I, auto J) { constexpr int S = decltype(I)::v, H = decltype(J)::v;
                  if constexpr (S < 4 || H == 0) lda<S < 4 ? 8 + 2 * S + H : 12 + S>(c, a, c.vl); });
    STAMP(9);
    u64x4 f4;
    p.run<8, 20>(a, [&](auto I) { constexpr int S = decltype(I)::v; waitv<19 - S + (S > 8), 300 + S>(a[S]); },
                 [&](auto I, auto J) { constexpr int S = decltype(I)::v, H = decltype(J)::v;
                   if constexpr (S == 8 && H == 0) publish<7>(c.tag7, c.vpub, c.rw);
                   if constexpr (S == 9 && H == 0) mk_gate();
                   if constexpr (S == 12 && H == 0) poll(f4, ws, c.sfl); });
    polled(f4);
    STAMP(10);
    if (__builtin_expect(!ready<4>(f4, c.tag7), 0)) spin<u64x4, 4>(ws, c.sfl, c.tag7);
    poll(f4, ws, c.sfl + 32);
    sfor<20, 28>([&](auto I) { lda<decltype(I)::v>(c, a, c.vl); });
    polled(f4);
    STAMP(11);
    if (__builtin_expect(!ready<4>(f4, c.tag7), 0)) spin<u64x4, 4>(ws, c.sfl + 32, c.tag7);
    STAMP(12);
    p.run<20, 36>(a, [&](auto I) { constexpr int S = decltype(I)::v;
                   if constexpr (S == 20) { lda<28>(c, a, c.vl); lda<29>(c, a, c.vl); }
                   waitv<(S < 23 ? 29 + 2 * (S - 20) : 35) - S, 300 + S>(a[S]); },
                 [&](auto I, auto J) { constexpr int S = decltype(I)::v, H = decltype(J)::v;
                   if constexpr (S < 23) lda<30 + 2 * (S - 20) + H>(c, a, c.vl);
                   scale_a(I, J); });
    mix();
    STAMP(13);
    u32x4 t;
#define PK8(acc, o) _Pragma("unroll") for (int i = 0; i < 4; ++i) t[i] = pk2(acc[o + 2 * i], acc[o + 2 * i + 1]);
    PK8(r0, 0) st128<350, 0>(t, c.vl, c.rw, spo);
    PK8(r0, 8) st128<351, 1024>(t, c.vl, c.rw, spo);
    PK8(r1, 0) st128<352, 2048>(t, c.vl, c.rw, spo);
    PK8(r1, 8) st128<353, 3072>(t, c.vl, c.rw, spo);
    const u32 vp = c.vl == 0 ? sfp : OOB;
    drain();
    publish<1>(c.tag7, vp, c.rw);
    STAMP(14);
    if (b == 0 && c.w == 4) {
      const u32 ln = c.vl >> 4, ve = fprog(ln >> 4, 0, (ln >> 3) & 1, ln & 7, 0);
      for (u32 n = 0; n < SPIN_CAP; ++n) {
        u32x2 fe[4];
#pragma unroll
        for (int i = 0; i < 4; ++i) ld64<8>(fe[i], ve + i * 8192, c.rw);
        drain();
        bool ok = true;
#pragma unroll
        for (int i = 0; i < 4; ++i) { tie(fe[i]); ok &= ready<2>(((u64)fe[i].y << 32) | fe[i].x, c.tag7); }
        if (__all(ok)) break;
      }
      publish<0>(c.tag7, c.vl == 0 ? WS_E : OOB, c.rw);
    }
  } else {
    p.run<0, 8>(a, none, none2);
    drain();
    publish<7>(c.tag7, c.vpub, c.rw);
    if (__builtin_expect(!mid, 0)) { STAMP(28); spin<u64x8, 2>(ws, c.sfl, c.tag7); }
    STAMP(9);
    ld_gate();
    sfor<8, 20>([&](auto I) { lda<decltype(I)::v>(c, a, c.vl); });
    p.run<8, 14>(a, [&](auto I) { constexpr int S = decltype(I)::v; waitv<19 - S, 300 + S>(a[S]); },
                 [&](auto I, auto J) { constexpr int S = decltype(I)::v, H = decltype(J)::v;
                   if constexpr (S == 8 && H == 0) poll(fl, ws, c.sfl);
                   if constexpr (S == 9 && H == 0) mk_gate(); });
    polled(fl);
    STAMP(10);
    if (__builtin_expect(!ready<4>(fl, c.tag7), 0)) spin<u64x8, 4>(ws, c.sfl, c.tag7);
    STAMP(11);
    u64 fp;
    u32x4 pp[4];
    bool pok;
    const u32 spi = spo ^ 16384;
    p.run<14, 36>(a, [&](auto I) { constexpr int S = decltype(I)::v;
                   if constexpr (S < 20) waitv<19 - S + 2 * (S - 14), 300 + S>(a[S]);
                   else waitv<(S < 22 ? 31 + 2 * (S - 20) : 35) - S + (S > 33 ? 4 : 0), 300 + S>(a[S]); },
                 [&](auto I, auto J) { constexpr int S = decltype(I)::v, H = decltype(J)::v;
                   if constexpr (S < 22) lda<20 + 2 * (S - 14) + H>(c, a, c.vl);
                   if constexpr (S == 26 && H == 0) poll(fp, ws, sfp);
                   if constexpr (S == 33 && H == 1) {
                     polled(fp); pok = ready<1>(fp, c.tag7);
                     const u32 v = pok ? c.vl : OOB;
                     ld128<360, 0>(pp[0], v, c.rw, spi); ld128<361, 1024>(pp[1], v, c.rw, spi); ld128<362, 2048>(pp[2], v, c.rw, spi); ld128<363, 3072>(pp[3], v, c.rw, spi);
                   }
                   scale_a(I, J); });
    mix();
    STAMP(13);
    drain();
    tie(pp[0]); tie(pp[1]); tie(pp[2]); tie(pp[3]);
    if (!pok) {
      spin<u64, 1>(ws, sfp, c.tag7);
      STAMP(12);
      rld128<0>(pp[0], c.vl, c.rw, spi); rld128<1024>(pp[1], c.vl, c.rw, spi); rld128<2048>(pp[2], c.vl, c.rw, spi); rld128<3072>(pp[3], c.vl, c.rw, spi);
      drain();
      tie(pp[0]); tie(pp[1]); tie(pp[2]); tie(pp[3]);
    }
    STAMP(14);
    const u32 sc = lbase + L_R3 + c.m * 4096 + c.vl, sr = sc ^ 8192;
#pragma unroll
    for (int i = 0; i < 4; ++i) {
      const u32 u0 = pp[2 + (i >> 1)][2 * (i & 1)], u1 = pp[2 + (i >> 1)][2 * (i & 1) + 1];
      *(LAS f32x4*)(sc + i * 1024) = f32x4{r1[4 * i] + u2f(u0 << 16), r1[4 * i + 1] + u2f(u0 & 0xFFFF0000u), r1[4 * i + 2] + u2f(u1 << 16), r1[4 * i + 3] + u2f(u1 & 0xFFFF0000u)};
    }
#pragma unroll
    for (int i = 0; i < 8; ++i) { const u32 u0 = pp[i >> 2][i & 3]; r0[2 * i] += u2f(u0 << 16); r0[2 * i + 1] += u2f(u0 & 0xFFFF0000u); tie(r0[2 * i]); tie(r0[2 * i + 1]); }
    bar_l();
#pragma unroll
    for (int i = 0; i < 4; ++i) {
      const f32x4 v = *(LAS f32x4*)(sr + i * 1024);
#pragma unroll
      for (int k = 0; k < 4; ++k) r0[4 * i + k] += v[k];
    }
    const rsrc_t ro = __builtin_amdgcn_make_buffer_rsrc(out, 0, 2048 * 1024, 0x00020000);
    const u32 vo = ((c.vl << 6) & 0x7C00) + ((c.vl >> 5) & 16);
    u32x4 y[2];
#pragma unroll
    for (int i = 0; i < 2; ++i) {
      u32x4 t = {pk2(r0[8 * i], r0[8 * i + 1]), pk2(r0[8 * i + 2], r0[8 * i + 3]), pk2(r0[8 * i + 4], r0[8 * i + 5]), pk2(r0[8 * i + 6], r0[8 * i + 7])};
      nop2(t);
      const auto s0 = __builtin_amdgcn_permlane32_swap(t[0], t[2], false, false);
      const auto s1 = __builtin_amdgcn_permlane32_swap(t[1], t[3], false, false);
      y[i] = u32x4{s0[0], s1[0], s0[1], s1[1]};
    }
    asm volatile("buffer_store_dwordx4 %0, %2, %3, %4 offen\n\tbuffer_store_dwordx4 %1, %2, %3, %4 offen offset:32\n\ts_nop 1"
                 ::"v"(y[0]), "v"(y[1]), "v"(vo), "s"(ro), "s"(sy) : "memory");
  }
}

extern "C" void kernel_launch(void* const* d_in, const int* in_sizes, int n_in, void* d_out, int out_size, void* d_ws, size_t ws_size,
                              hipStream_t stream) {
  gated_mix_kernel<<<256, 512, 0, stream>>>(d_in[0], d_in[1], d_in[2], d_in[4], d_in[5], d_in[6], d_in[7], d_out, d_ws);
}
```

```cpp
#include <hip/hip_runtime.h>
#include <stdint.h>

typedef unsigned u32;
typedef unsigned long long u64;
typedef u32 u32x2 __attribute__((ext_vector_type(2)));
typedef u32 u32x4 __attribute__((ext_vector_type(4)));
typedef short s16x4 __attribute__((ext_vector_type(4)));
typedef __bf16 bf16x8 __attribute__((ext_vector_type(8)));
typedef float f32x2 __attribute__((ext_vector_type(2)));
typedef float f32x4 __attribute__((ext_vector_type(4)));
typedef float f32x16 __attribute__((ext_vector_type(16)));
typedef u64 u64x2 __attribute__((ext_vector_type(2)));
typedef u64 u64x4 __attribute__((ext_vector_type(4)));
typedef u64 u64x8 __attribute__((ext_vector_type(8)));
typedef u32 u32x3 __attribute__((ext_vector_type(3)));
typedef u32 u32x8 __attribute__((ext_vector_type(8)));
typedef u32 u32x16 __attribute__((ext_vector_type(16)));
typedef u32x4 rsrc_t;
#define LAS __attribute__((address_space(3)))
#define DI __device__ __forceinline__
#define SB() __builtin_amdgcn_sched_barrier(0)

#define EXP 0
#define ROTA 0
#define ROTB 0
#define ROTC 2
#define ROTD 4
#define SPINSLEEP 2
#define PD 3
#define TAGX 0
#define RP 2
#define RK 2
#define KVP 24
#define AP0 6
#define OCHK 18
#define OSLP 8
#define R3 1
#define CODE_LINES 198

struct Srd { const void* p; u32 n, f; };
struct Args { Srd x, ws, w2, wo, w1a, wqkv, w1b, b1, gw, out; };

namespace {

constexpr u32 WS_E = 0;
constexpr u32 WS_FPROG = 4096;
constexpr u32 WS_FKV = WS_FPROG + 256 * RP * 128;
constexpr u32 WS_FP = WS_FKV + 64 * RK * 128;
constexpr u32 NFL = RP + (RK + 3) / 4 + 2;
constexpr u32 WS_GATE = WS_FPROG + 256 * NFL * 128;
constexpr u32 WS_ZP = WS_GATE + 1024 * 128;
constexpr u32 WS_KVP = WS_ZP + 256 * 128;
constexpr u32 WS_HND = WS_KVP + 256 * 2048;
constexpr u32 WS_PART = WS_HND + 256 * 36864;
constexpr u32 WS_END = WS_PART + 2048 * 3072;
constexpr u32 WS_STAMP = 32u << 20;

constexpr u32 L_R0 = 0, L_R1 = 65536, L_R2 = 98304, L_R3 = 147456, L_END = 163840;
constexpr int NSLOT = PD + 2;
constexpr u32 RING_SLOT = 24576, L_RING = 139264 - NSLOT * RING_SLOT;
DI constexpr u32 ring(int pos) { return L_RING + ((pos + (PD == 3 ? 2 : 0)) % NSLOT) * RING_SLOT; }
constexpr u32 L_S1 = L_R0;
constexpr u32 L_GW = L_R3;
constexpr u32 L_S2 = L_R3;

constexpr u64 TAG_STEP = 0x9E3779B97F4A7C18ull;
constexpr u32 SPIN_CAP = 1u << 16;
constexpr u32 OOB = 0x80000000u;
constexpr u32 fprog(u32 r, u32 rt, u32 type, u32 j, u32 g) { return WS_FPROG + ((r * 4 + rt) * 4 + (j >> 1)) * RP * 128 + (type * 4 + (j & 1) * 2 + g) * 8; }

enum { T_SX, T_S1, T_SX0, T_SW0, T_SX1, T_SW1, T_SX2, T_SW2, T_ROT, T_BIAS, T_E0, T_E1, T_E2, T_L0, T_L1, T_L2,
       T_S3, T_ST, T_SB2, T_SBO, T_SIN, T_SO, T_SFL, T_PUB, T_SY, T_SG, T_SP0, T_SP1, T_AUX, T_FKV, T_KVP, T_ZP };
struct Tab {
  u32 v[2048][32];
  constexpr Tab() : v{} {
    for (u32 i = 0; i < 2048; ++i) {
      const u32 b = i >> 3, w = i & 7, m = w & 3, g = w >> 2, xc = b & 7, slot = b >> 3;
      const u32 type = slot >> 4, r1 = (xc >> 2) * 8 + (slot & 7), j = 2 * (xc & 3) + ((slot >> 3) & 1), jj = j & 1;
      const u32 rot = (ROTA * (slot & 7) + ROTB * (slot >> 3) + ROTC * (xc & 3) + ROTD * (xc >> 2)) & 7;
      const u32 p1 = type ? 3072 : 3584, p2 = type ? 3584 : 3072;
      const u32 cu0 = 448 * j + 192 * jj, cuU = 448 * j + 256 - 192 * jj, cuT = 448 * j + 384 - 384 * jj;
      const u32 cu1 = type ? 1024 + 128 * j : cu0, cu3 = type ? cuU : 128 * j;
      const u32 r2 = (xc >> 1) * 4 + (slot >> 3), ct = slot & 7, kh = xc & 1, kq = m >> 1, rt = 2 * (kh ^ g) + (m & 1);
      const u32 cons = (r2 * 4 + rt) * 4 + 2 * kh + kq, sout = WS_HND + ((r1 * 4 + m) * 4 + (j >> 1)) * 36864;
      u32* t = v[i];
      t[T_SX] = (r1 * 128 + 8 * w) * 1024; t[T_S1] = 8 * w * p1 + cu1; t[T_ROT] = rot;
      t[T_SX0] = t[T_SX] + rot * 128; t[T_SW0] = t[T_S1] + rot * 64 * p1;
      t[T_SX1] = t[T_SX] + ((rot + 1) & 7) * 128; t[T_SW1] = t[T_S1] + ((rot + 1) & 7) * 64 * p1;
      t[T_SX2] = t[T_SX] + ((rot + 2) & 7) * 128; t[T_SW2] = t[T_S1] + ((rot + 2) & 7) * 64 * p1;
      t[T_BIAS] = ((type ? cuU : cu0) + 64 * g) | (type ? cuT : cu0 + 128 + 64 * g) << 16;
      auto req = [&](u32 i0, u32 arg, u32 off, u32 pitch, u32 n, u32 delta) { t[i0] = off; t[i0 + 1] = delta; t[i0 + 2] = pitch | n << 16 | arg << 24; };
      const bool c3 = (xc & 3) == 1 && (R3 & 1);
      req(T_E0, 0, t[T_SX0], 0, 0, 0); req(T_L0, 16, WS_FPROG + b * NFL * 128, 0, 0, 0);
      if (w == 0) req(T_E0, 64 + type * 16, r1 * 32 * p1 + cu1 + (type ? 1024 : 128), p1, 32, 0);
      if (w == 1 && (R3 & 2)) req(T_E0, 0, (r1 * 128 + (slot >> 3) * 32) * 1024 + 384, 1024, 32, 0);
      if (w == 2 && c3) req(T_E0, 80, (slot >> 1) * 32 * 3072 + 1024 + (slot & 1) * 1024 + 384, 3072, 32, 0);
      if (w == 1) req(T_L0, 80 + type * 16, r1 * 32 * p2 + (type && jj ? cuT : cu3), p2, 32 + 32 * type, jj ? 64 : 128);
      if (w == 2) req(T_L0, 32, (896 * kh + r2 * 56) * 1024 + ct * 128, 1024, 56, 32768);
      if (w == 3) req(T_L0, 48, (256 * kh + r2 * 16) * 1024 + ct * 128, 1024, 16, 0);
      if (w == 4) req(T_L0, 16, WS_FPROG + b * NFL * 128, 128, NFL, 0);
      if (w == 5 && (R3 & 1)) req(T_L0, 32, (896 * kh + slot * 28) * 1024 + 384, 1024, 28, 0);
      if (w == 6 && (R3 & 1)) req(T_L0, 48, (256 * kh + slot * 8) * 1024 + 384, 1024, 8, 0);
      if (w == 7 && c3) req(T_L0, 80, slot * 16 * 3072 + 384, 3072, 16, 0);
      t[T_S3] = 8 * w * p2 + cu3; t[T_ST] = 16 * m * 3584 + cuT;
      t[T_SB2] = (896 * kh + 8 * w) * 1024 + ct * 128; t[T_SBO] = (256 * kh + 8 * w) * 1024 + ct * 128;
      t[T_SIN] = WS_HND + cons * 36864; t[T_SO] = sout + jj * 4096 + g * 2048;
      t[T_SFL] = WS_FPROG + (cons * RP + ct % RP) * 128; t[T_PUB] = fprog(r1, m, type, j, g);
      t[T_SY] = (r2 * 128 + rt * 32) * 1024 + ct * 128 + kq * 64; t[T_SG] = WS_GATE + cons * 512;
      t[T_SP0] = WS_PART + (((b ^ 1 ^ g) * 4 + m) * 2) * 3072; t[T_SP1] = WS_PART + (((b ^ 1 ^ g) * 4 + (g ? m : m ^ 2)) * 2 + 1) * 3072;
      t[T_AUX] = type ? sout + 16384 + jj * 2048 + g * 1024 : WS_GATE + (((r1 * 4 + m) * 8 + j) * 2 + g) * 128;
      t[T_FKV] = WS_FKV + (j * 8 + w) * RK * 128 + (type ? r1 * 4 : (r1 % RK) * 128);
      t[T_KVP] = WS_KVP + ((j * 2 + g) * 16 + (type ? r1 : 0)) * 2048 + m * 512; t[T_ZP] = WS_ZP + ((j * 2 + g) * 16 + (type ? r1 : 0)) * 128;
    }
  }
};
__device__ __attribute__((aligned(128))) const Tab tab{};

DI u32 f2u(float f) { return __builtin_bit_cast(u32, f); }
DI float u2f(u32 u) { return __builtin_bit_cast(float, u); }
template <int I> struct IC { static constexpr int v = I; };
template <int B, int E, class F> DI void sfor(F&& f) { if constexpr (B < E) { f(IC<B>{}); sfor<B + 1, E>(f); } }
DI u32 fresh_lane() { u32 l; asm volatile("v_mbcnt_lo_u32_b32 %0, -1, 0\n\tv_mbcnt_hi_u32_b32 %0, -1, %0" : "=v"(l)); return l; }
DI f32x16 mfma(u32x4 a, u32x4 b, f32x16 c) {
  return __builtin_amdgcn_mfma_f32_32x32x16_bf16(__builtin_bit_cast(bf16x8, a), __builtin_bit_cast(bf16x8, b), c, 0, 0, 0);
}
DI void tie0(u32& a) { asm("" : "+v"(a)); }
DI u32 pk2(float lo, float hi) { u32 r; asm("v_cvt_pk_bf16_f32 %0, %1, %2" : "=v"(r) : "v"(lo), "v"(hi)); return r; }
DI u32 vsel(bool c, u32 a, u32 b) { tie0(a); tie0(b); return c ? a : b; }
DI float bfi(u32 m, float a, float b) { float r; asm("v_bfi_b32 %0, %1, %2, %3" : "=v"(r) : "v"(m), "v"(a), "v"(b)); return r; }
DI void nop2(u32x4& a) { asm volatile("s_nop 1" : "+v"(a)); }
DI u32x4 lds128(u32 a) { return *(LAS u32x4*)a; }
DI u32x2 ldstr(u32 a) { return __builtin_bit_cast(u32x2, __builtin_amdgcn_ds_read_tr16_b64_v4i16((LAS s16x4*)a)); }
DI u32x4 ldstr2(u32 a0, u32 a1) { u32x2 lo = ldstr(a0), hi = ldstr(a1); return u32x4{lo.x, lo.y, hi.x, hi.y}; }

template <int ID, u32 L, u32 K> DI void dma(u32 wl, u32 voff, rsrc_t rs, u32 sbase) {
  u32 t;
  asm volatile("s_add_u32 m0, %1, %5\n\ts_add_u32 %0, %4, %6\n\tbuffer_load_dwordx4 %2, %3, %0 offen lds ; op %7"
               : "=&s"(t) : "s"(wl), "v"(voff), "s"(rs), "s"(sbase), "i"(L), "i"(K), "i"(ID) : "memory", "scc");
}
template <int ID, int OFF> DI void ld128(u32x4& d, u32 voff, rsrc_t rs, u32 soff) {
  asm volatile("buffer_load_dwordx4 %0, %1, %2, %3 offen offset:%4 sc1 ; op %5" : "=v"(d) : "v"(voff), "s"(rs), "s"(soff), "i"(OFF), "i"(ID) : "memory");
}
template <int OFF> DI void rld128(u32x4& d, u32 voff, rsrc_t rs, u32 soff) {
  asm volatile("buffer_load_dwordx4 %0, %1, %2, %3 offen offset:%4 sc1" : "+v"(d) : "v"(voff), "s"(rs), "s"(soff), "i"(OFF) : "memory");
}
template <int ID, int OFF> DI void ld96(u32x3& d, u32 voff, rsrc_t rs, u32 soff) {
  asm volatile("buffer_load_dwordx3 %0, %1, %2, %3 offen offset:%4 sc1 ; op %5" : "=v"(d) : "v"(voff), "s"(rs), "s"(soff), "i"(OFF), "i"(ID) : "memory");
}
template <int OFF> DI void rld96(u32x3& d, u32 voff, rsrc_t rs, u32 soff) {
  asm volatile("buffer_load_dwordx3 %0, %1, %2, %3 offen offset:%4 sc1" : "+v"(d) : "v"(voff), "s"(rs), "s"(soff), "i"(OFF) : "memory");
}
template <int ID> DI void ld64(u32x2& d, u32 voff, rsrc_t rs) {
  asm volatile("buffer_load_dwordx2 %0, %1, %2, 0 offen sc1 ; op %3" : "=v"(d) : "v"(voff), "s"(rs), "i"(ID) : "memory");
}
template <int ID, int OFF> DI void ld32(u32& d, u32 voff, rsrc_t rs, u32 soff) {
  asm volatile("buffer_load_dword %0, %1, %2, %3 offen offset:%4 sc1 ; op %5" : "=v"(d) : "v"(voff), "s"(rs), "s"(soff), "i"(OFF), "i"(ID) : "memory");
}
template <int ID, int OFF> DI void st128(u32x4 d, u32 voff, rsrc_t rs, u32 soff) {
  asm volatile("buffer_store_dwordx4 %0, %1, %2, %3 offen offset:%4 sc1 ; op %5\n\ts_nop 1" ::"v"(d), "v"(voff), "s"(rs), "s"(soff), "i"(OFF), "i"(ID) : "memory");
}
template <int ID> DI void st64(u32x2 d, u32 voff, rsrc_t rs) {
  asm volatile("buffer_store_dwordx2 %0, %1, %2, 0 offen sc1 ; op %3" ::"v"(d), "v"(voff), "s"(rs), "i"(ID) : "memory");
}
template <int ID> DI void st32(u32 d, u32 voff, rsrc_t rs, u32 soff) {
  asm volatile("buffer_store_dword %0, %1, %2, %3 offen sc1 ; op %4" ::"v"(d), "v"(voff), "s"(rs), "s"(soff), "i"(ID) : "memory");
}
template <int N, int NEED> DI void waitv() { asm volatile("s_waitcnt vmcnt(%0) ; need %1" ::"i"(N), "i"(NEED) : "memory"); }
template <int N, int NEED> DI void waitv(u32x2& a) { asm volatile("s_waitcnt vmcnt(%1) ; need %2" : "+v"(a) : "i"(N), "i"(NEED) : "memory"); }
template <int N, int NEED> DI void waitv(u32x4& a) { asm volatile("s_waitcnt vmcnt(%1) ; need %2" : "+v"(a) : "i"(N), "i"(NEED) : "memory"); }
DI void drain() { asm volatile("s_waitcnt vmcnt(0)" ::: "memory"); }
DI void tie(u32x4& a) { asm volatile("" : "+v"(a)); }
DI void tie(u32x3& a) { asm volatile("" : "+v"(a)); }
DI void tie(u32x2& a) { asm volatile("" : "+v"(a)); }
DI void tie(u32& a) { asm volatile("" : "+v"(a)); }
DI void tie(float& a) { asm volatile("" : "+v"(a)); }
DI void tie(f32x16& a) { asm volatile("" : "+v"(a)); }
template <int N, int NEED> DI void bar_v() { SB(); asm volatile("s_waitcnt vmcnt(%0) lgkmcnt(0) ; need %1\n\ts_barrier" ::"i"(N), "i"(NEED) : "memory"); SB(); }
template <int N, int NEED> DI void bar_w() { SB(); asm volatile("s_waitcnt vmcnt(%0) ; need %1\n\ts_barrier" ::"i"(N), "i"(NEED) : "memory"); SB(); }
DI void bar() { SB(); asm volatile("s_barrier" ::: "memory"); SB(); }
DI void bar_l() { SB(); asm volatile("s_waitcnt lgkmcnt(0)\n\ts_barrier" ::: "memory"); SB(); }

DI void poll(u64x2& f, u64 ws, u32 off) { asm volatile("s_load_dwordx4 %0, %1, %2 glc ; sop" : "=s"(f) : "s"(ws), "s"(off) : "memory"); }
DI void poll(u64x4& f, u64 ws, u32 off) { asm volatile("s_load_dwordx8 %0, %1, %2 glc ; sop" : "=s"(f) : "s"(ws), "s"(off) : "memory"); }
DI void poll(u64x8& f, u64 ws, u32 off) { asm volatile("s_load_dwordx16 %0, %1, %2 glc ; sop" : "=s"(f) : "s"(ws), "s"(off) : "memory"); }
template <class T> DI void polled(T& f) { asm volatile("s_waitcnt lgkmcnt(0)" : "+s"(f) :: "memory"); }
template <u32 BIT> DI bool match(u64 all, u64 any, u64 tag7) { return (EXP & 16) || (((all | (7u & ~BIT)) ^ tag7) | ((any | 7u) ^ tag7)) == 0; }
template <u32 BIT> DI bool ready(u64 f, u64 tag7) { return (EXP & 16) || (f | (7u & ~BIT)) == tag7; }
template <u32 BIT> DI bool ready(u64x2 f, u64 tag7) { return match<BIT>(f[0] & f[1], f[0] | f[1], tag7); }
template <u32 BIT> DI bool ready(u64x4 f, u64 tag7) { return match<BIT>((f[0] & f[1]) & (f[2] & f[3]), (f[0] | f[1]) | (f[2] | f[3]), tag7); }
template <u32 BIT> DI bool ready(u64x8 f, u64 tag7) {
  return match<BIT>(((f[0] & f[1]) & (f[2] & f[3])) & ((f[4] & f[5]) & (f[6] & f[7])), ((f[0] | f[1]) | (f[2] | f[3])) | ((f[4] | f[5]) | (f[6] | f[7])), tag7);
}
template <u32 BITS> DI void publish(u64 tag7, u32 voff, rsrc_t rw) { st64<9>(u32x2{(u32)tag7 ^ (7u & ~BITS), (u32)(tag7 >> 32)}, voff, rw); }
template <class T, u32 BIT> DI void spin(u64 ws, u32 off, u64 tag7) {
#pragma unroll 1
  for (u32 n = 0; n < SPIN_CAP; ++n) {
    T f;
    poll(f, ws, off);
    polled(f);
    if (ready<BIT>(f, tag7)) break;
    __builtin_amdgcn_s_sleep(SPINSLEEP);
  }
}

#define STAMP(i) do {} while (0)
#define STAMP_ARG
#define STAMP_PASS

struct Ctx {
  u32 w, m, g, jj;
  u32x16 t0, t1;
  template <int I> DI u32 t() const { return I < 16 ? t0[I & 15] : t1[I & 15]; }
  u64 tag7, ws;
  rsrc_t rx, rW1, rW2, rw2, rwo, rw;
  u32 wl;
  u32 vx, vW1, vW2, vB;
  u32 ax, ax3;
  u32 aw[3][4];
  u32 vl, vpub;
};

template <int I> DI void dmaX0(const Ctx& c) {
  dma<200 + I, L_R0 + I * 8192, (448 * (I >> 2) + ((I >> 1) & 1) * 320 + (I & 1) * 64) * 1024>(c.wl, c.vB, c.rw2, c.t<T_SB2>());
}
template <int I> DI void dmaX1(const Ctx& c) {
  dma<210 + I, L_R2 + I * 8192, (448 * (I / 3) + 128 + (I % 3) * 64) * 1024>(c.wl, c.vB, c.rw2, c.t<T_SB2>());
}
template <int I> DI void dmaX2(const Ctx& c) { dma<220 + I, L_R1 + I * 8192, I * 65536>(c.wl, c.vB, c.rwo, c.t<T_SBO>()); }
template <int I> DI void lda(const Ctx& c, u32x4* a, u32 v) { ld128<300 + I, (I & 3) * 1024>(a[I], v, c.rw, c.t<T_SIN>() + (I >> 2) * 4096); }

template <int TYPE> struct T1 {
  static constexpr u32 P1 = TYPE ? 3072 : 3584, P2 = TYPE ? 3584 : 3072;
  static constexpr u32 D12 = TYPE ? 1024 : 128;
  template <int POS> static DI void dmaW2(const Ctx& c) { dma<100 + POS, L_R0 + POS * 8192, D12>(c.wl, c.vW1, c.rW1, c.t<T_S1>() + ((c.t<T_ROT>() + POS) & 7) * (64 * P1)); }
  template <int POS> static DI void dmaW3(const Ctx& c) { dma<110 + POS, L_R2 + POS * 8192, 0>(c.wl, c.vW2, c.rW2, c.t<T_S3>() + ((c.t<T_ROT>() + POS) & 7) * (64 * P2)); }
  template <int IT, int I> static DI void later(const Ctx& c) {
    constexpr int POS = IT + PD;
    if constexpr (POS < 8) {
      const u32 kc = (c.t<T_ROT>() + POS) & 7;
      if constexpr (I < 2) dma<10 * POS + 10 + I, ring(POS) + I * 8192, I * 65536>(c.wl, c.vx, c.rx, c.t<T_SX>() + kc * 128);
      else dma<10 * POS + 10 + I, ring(POS) + 16384, 0>(c.wl, c.vW1, c.rW1, c.t<T_S1>() + kc * (64 * P1));
    } else if constexpr ((POS - 8) * 3 + I < 8) dmaW2<(POS - 8) * 3 + I>(c);
  }
};
template <int TYPE> DI f32x16 mm1(u32x4 wf, u32x4 xf, f32x16 acc) { acc = TYPE ? mfma(xf, wf, acc) : mfma(wf, xf, acc); tie(acc); return acc; }
DI f32x16 add_bias(u32 bias, u32 sel, f32x16 acc) { return mm1<0>(u32x4{bias, 0u, 0u, 0u}, u32x4{sel, 0u, 0u, 0u}, acc); }
DI u32x4 wfrag(u32 a) { return ldstr2(a, a + 512); }
template <int TYPE, int W> DI u32x4 wstep(const Ctx& c, u32 off, int s) {
  return TYPE ? wfrag(c.aw[W][s & 2] + off + (s & ~2) * 2048) : wfrag(c.aw[W][s & 3] + off + (s & ~3) * 2048);
}
template <int TYPE> DI void lds_bases(Ctx& c, u32 X) {
  const u32 lane = c.vl >> 4, h = lane >> 5, row = 32 * c.m + (lane & 31);
  c.ax = L_RING + row * 128 + (((h + 2 * X) ^ ((row >> 1) & 7)) * 16);
  c.ax3 = c.ax + 3 * RING_SLOT;
  tie(c.ax); tie(c.ax3);
  const u32 q = (lane & 15) >> 2, pp = lane & 3, blk = (lane >> 4) & 1;
  const u32 a = (8 * h + q) * 128 + (((4 * c.g + 2 * blk + (pp >> 1)) ^ ((q >> 1) << 2)) * 16) + 8 * (pp & 1) + X * 2048;
#pragma unroll
  for (int t = 0; t < 4; t += 1 + TYPE) {
    c.aw[0][t] = a ^ (t * 2048); c.aw[1][t] = c.aw[0][t] + L_R1; c.aw[2][t] = c.aw[0][t] + L_R2;
    tie(c.aw[0][t]); tie(c.aw[1][t]); tie(c.aw[2][t]);
  }
}

constexpr int NPRO = 2, NTOUCH = 0, NLATE = 1;
constexpr int NW2 = PD == 3 ? 8 : 6;
template <int TYPE, class G, class F>
DI void pass1(const Ctx& c, u32x4 (&xf)[32], f32x16& acc, f32x16& aG, u32 ag, u32x4 (&fu)[4], G&& second, F&& side STAMP_ARG) {
  typedef T1<TYPE> P;
  u32x4 wf[4], gf;
  auto xaddr = [&](int pos, u32 x) { const u32 o = ring(pos) - L_RING; return o >= 3 * RING_SLOT ? (c.ax3 ^ x) + (o - 3 * RING_SLOT) : (c.ax ^ x) + o; };
  auto rd = [&](int pos, int t) {
    const u32 wo = ring(pos) + 16384;
    xf[4 * pos + t] = lds128(xaddr(pos, t << 5));
    wf[t] = wo < L_R1 ? wstep<TYPE, 0>(c, wo, t) : wo < L_R2 ? wstep<TYPE, 1>(c, wo - L_R1, t) : wstep<TYPE, 2>(c, wo - L_R2, t);
    SB();
  };
  rd(0, 0); rd(0, 1);
  second(false);
  P::template later<0, 0>(c); SB();
  sfor<0, 8>([&](auto I) {
    constexpr int pos = decltype(I)::v;
    rd(pos, 2);
    if constexpr (pos == 0) acc = mm1<TYPE>(wf[0], xf[0], f32x16{}); else acc = mm1<TYPE>(wf[0], xf[4 * pos], acc);
    SB();
    rd(pos, 3);
    if constexpr (TYPE == 0) { gf = lds128(ag + pos * 128); SB(); }
    P::template later<pos, 1>(c); SB();
    acc = mm1<TYPE>(wf[1], xf[4 * pos + 1], acc); SB();
    P::template later<pos, 2>(c); SB();
    if constexpr (pos == 7 && NLATE) { second(true); SB(); }
    if constexpr (pos < 7) bar_w<3 * (PD - 1) + (pos + 1 < PD ? NPRO : 0) + (pos == 0 ? NTOUCH : 0), 10 * (pos + 1) + 12>(); else bar_w<NW2 - 3 + NLATE, 102>();
    STAMP(16 + pos);
    if constexpr (pos < 7) rd(pos + 1, 0); else { fu[0] = wstep<TYPE, 0>(c, 0, 0); SB(); }
    acc = mm1<TYPE>(wf[2], xf[4 * pos + 2], acc); SB();
    if constexpr (pos < 7) rd(pos + 1, 1); else { fu[1] = wstep<TYPE, 0>(c, 0, 1); fu[2] = wstep<TYPE, 0>(c, 0, 2); SB(); }
    acc = mm1<TYPE>(wf[3], xf[4 * pos + 3], acc); SB();
    if constexpr (TYPE == 0) { if constexpr (pos == 0) aG = mm1<0>(gf, xf[0], f32x16{}); else aG = mm1<0>(gf, xf[4 * pos], aG); SB(); }
    side(I);
    if constexpr (pos < 7) { P::template later<pos + 1, 0>(c); SB(); }
  });
}

template <int NS, int SWAP, bool ZERO, class PRE, class RD, class POST>
DI void tile(u32x4 (&xf)[32], f32x16& acc, u32x4 (&fu)[4], PRE&& pre, RD&& rd, POST&& post) {
  sfor<0, NS>([&](auto I) {
    constexpr int s = decltype(I)::v;
    pre(I);
    rd(IC<s + 3>{}, fu[(s + 3) % 4]); SB();
    constexpr int xi = NS == 32 ? s : 4 * (s >> 1) + (s & 1);
    if constexpr (ZERO && s == 0) acc = mm1<SWAP>(fu[0], xf[0], f32x16{}); else acc = mm1<SWAP>(fu[s % 4], xf[xi], acc);
    SB();
    post(I); SB();
  });
}

template <int SL, int NSL, int V, class S> DI void mish4(const S& h, float (&e)[4], u32* out) {
  constexpr int cut[2][9] = {{0, 5, 8, 14, 18, 22, 26, 26, 26}, {0, 4, 6, 8, 12, 16, 18, 20, 26}};
  sfor<cut[NSL == 8][SL], cut[NSL == 8][SL + 1]>([&](auto I) {
    constexpr int o = decltype(I)::v, k = o & 3;
    if constexpr (o < 4) e[k] = __builtin_fmaf(h[V + k], 1.44269504f, -0.5f);
    else if constexpr (o < 8) e[k] = __builtin_amdgcn_exp2f(e[k]);
    else if constexpr (o < 12) e[k] += 0.70710678f;
    else if constexpr (o < 16) e[k] = __builtin_fmaf(e[k], e[k], 0.5f);
    else if constexpr (o < 20) e[k] = __builtin_amdgcn_rcpf(e[k]);
    else if constexpr (o < 24) e[k] = __builtin_fmaf(-h[V + k], e[k], h[V + k]);
    else out[k] = pk2(e[2 * k], e[2 * k + 1]);
    SB();
  });
}
template <int SL, int V> DI void phi4(const f32x16& t, float (&e)[8], u32* out) {
  constexpr int cut[7] = {0, 4, 6, 8, 12, 16, 22};
  sfor<cut[SL], cut[SL + 1]>([&](auto I) {
    constexpr int o = decltype(I)::v, k = o & 3;
    if constexpr (o < 4) e[k] = t[V + k] * 1.44269504f;
    else if constexpr (o < 8) e[k] = __builtin_amdgcn_fmed3f(__builtin_amdgcn_exp2f(e[k]), 0.f, 1.f);
    else if constexpr (o < 12) e[4 + k] = t[V + k] + 1e-6f;
    else if constexpr (o < 16) e[4 + k] = __builtin_fmaxf(e[4 + k], 1e-6f);
    else if constexpr (o < 20) e[k] += e[4 + k];
    else out[k] = pk2(e[2 * k], e[2 * k + 1]);
    SB();
  });
}

struct Early { u32 pf, pw, bias; };
DI u32 take_early(Early& e) { tie(e.pf); tie(e.bias); SB(); return e.bias; }
DI u32x4 vec4(const u32* p) { return u32x4{p[0], p[1], p[2], p[3]}; }

DI u32x4 mish8(const f32x16& a, int o) {
  f32x2 h[4], e[4];
  u32x4 r;
#pragma unroll
  for (int i = 0; i < 4; ++i) { h[i] = f32x2{a[o + 2 * i], a[o + 2 * i + 1]}; e[i] = __builtin_elementwise_fma(h[i], f32x2{1.44269504f, 1.44269504f}, f32x2{-0.5f, -0.5f}); }
#pragma unroll
  for (int i = 0; i < 4; ++i) e[i] = f32x2{__builtin_amdgcn_exp2f(e[i].x), __builtin_amdgcn_exp2f(e[i].y)};
#pragma unroll
  for (int i = 0; i < 4; ++i) { e[i] += f32x2{0.70710678f, 0.70710678f}; e[i] = __builtin_elementwise_fma(e[i], e[i], f32x2{0.5f, 0.5f}); }
#pragma unroll
  for (int i = 0; i < 4; ++i) e[i] = f32x2{__builtin_amdgcn_rcpf(e[i].x), __builtin_amdgcn_rcpf(e[i].y)};
#pragma unroll
  for (int i = 0; i < 4; ++i) { e[i] = __builtin_elementwise_fma(-h[i], e[i], h[i]); r[i] = pk2(e[i].x, e[i].y); }
  return r;
}

template <class F>
DI bool type_b(Ctx& c, Early& early, u32x4* a, bool& mid, F&& second STAMP_ARG) {
  typedef T1<0> P;
  u32x4 xf[32], fu[4];
  f32x16 a0, a1, aQ, aG;
  const u32 lane = c.vl >> 4, h = lane >> 5, s2 = L_S2 + c.g * 1088;
  const u32 gq = c.jj * 2 + c.g;
  lds_bases<0>(c, gq);
  u32 ag = vsel((lane & 31) == 0, L_GW + c.t<T_ROT>() * 128 + gq * 32 + h * 16, L_GW + 2048), one = h ? 0u : 0x3F80u;
  u32 vg = lane * 4 | h << 31, vp = h * 2048 + (lane & 31) * 16;
  u32 aA0 = vsel(lane & 16, s2 + 1024 + h * 16, s2 + c.vl), aA1 = vsel(lane & 16, s2 + c.vl, s2 + 1056 + h * 16);
  u32 aS = s2 + ((lane & 31) + 32 * (c.m & 1)) * 16 + (c.m >> 1) * 8 + h * 4;
  tie(ag); tie(one); tie(vg); tie(vp); tie(aA0); tie(aA1); tie(aS);
  bar_w<3, 12>();
  STAMP(2);
  pass1<0>(c, xf, a0, aG, ag, fu, second, [&](auto I) {
    if constexpr (decltype(I)::v == 2) {
      const u32 bias = take_early(early);
      a0 = add_bias(bias, one, a0); a1 = add_bias(bias, one ^ 0x3F80u, f32x16{}); SB();
    } } STAMP_PASS);
  STAMP(3);
  const u32 so = c.t<T_SO>();
  float e[4];
  u32 pk[4];
  static_assert(PD == 3, "schedule below");
  tile<32, 0, false>(xf, a1, fu,
    [&](auto I) { constexpr int s = decltype(I)::v;
      if constexpr (s == 9) bar_w<6 + NLATE, 107>();
      if constexpr (s == 23) bar();
      if constexpr (s == 29) { bar_w<4, 117>(); tie(early.pw); } },
    [&](auto I, u32x4& f) { constexpr int s = decltype(I)::v; if constexpr (s < 32) f = wstep<0, 0>(c, 0, s); else f = wstep<0, 2>(c, 0, s - 32); },
    [&](auto I) { constexpr int s = decltype(I)::v;
      if constexpr (s >= 1 && s <= 24) mish4<(s - 1) % 6, 6, 4 * ((s - 1) / 6)>(a0, e, pk + 2 * (((s - 1) / 6) & 1));
      if constexpr (s < 24 && !(s & 1)) { if constexpr (s < 8) dmaX2<s / 2>(c); else P::dmaW3<s / 2 - 4>(c); }
      if constexpr (s == 2) tie(aG);
      if constexpr (s == 7) st32<6>(f2u(aG[0]), vg, c.rw, c.t<T_AUX>());
      if constexpr (s == 13) st128<1, 0>(vec4(pk), c.vl, c.rw, so);
      if constexpr (s == 24) st128<2, 1024>(vec4(pk), c.vl, c.rw, so);
      if constexpr (s == 25 || s == 26 || s == 28) dmaX0<s == 28 ? 2 : s - 25>(c);
      if constexpr (s == 30) dmaX0<3>(c); });
  STAMP(4);
  bool st0;
  u32 v0;
  u64x4 f4;
  u64x8 fk;
  tile<32, 0, true>(xf, aQ, fu,
    [&](auto I) { constexpr int s = decltype(I)::v;
      if constexpr (s == 9 || s == 17 || s == 25) bar();
      if constexpr (s == 4) { waitv<6, 2>(); publish<1>(c.tag7, c.vpub, c.rw); } },
    [&](auto I, u32x4& f) { constexpr int s = decltype(I)::v; if constexpr (s < 32) f = wstep<0, 2>(c, 0, s); },
    [&](auto I) { constexpr int s = decltype(I)::v;
      if constexpr (s >= 1) mish4<(s - 1) % 8, 8, 4 * ((s - 1) / 8)>(a1, e, pk + 2 * (((s - 1) / 8) & 1));
      if constexpr (s == 0 || s == 2 || s == 6) dmaX0<s == 6 ? 6 : 4 + s / 2>(c);
      if constexpr (s == 9) dmaX0<7>(c);
      if constexpr (s == 11 || s == 13) dmaX1<(s - 11) / 2>(c);
      if constexpr (s == 14) poll(f4, c.ws, c.t<T_SFL>());
      if constexpr (s == 17) st128<3, 0>(vec4(pk), c.vl, c.rw, so + 8192);
      if constexpr (s == 18 || s == 19) dmaX1<s - 16>(c);
      if constexpr (s == 21) { polled(f4); st0 = ready<1>(f4, c.tag7); v0 = st0 ? c.vl : OOB; }
      if constexpr (s == 21 || s == 22 || s == 24) lda<s == 24 ? 2 : s - 21>(c, a, v0);
      if constexpr (s == 25 || s == 27) dmaX1<4 + (s - 25) / 2>(c);
      if constexpr (s == 29 || s == 30) lda<s - 26>(c, a, v0);
      if constexpr (s == KVP) poll(fk, c.ws, c.t<T_FKV>()); });
  mish4<7, 8, 12>(a1, e, pk + 2);
  st128<4, 1024>(vec4(pk), c.vl, c.rw, so + 8192);
  STAMP(5);
  polled(fk);
  {
    const u64 t2 = ((c.tag7 & 0xFFFFFFFFull) << 32) | (c.tag7 & 0xFFFFFFFFull);
    if (__builtin_expect(!ready<7>(fk, t2), 0)) { STAMP(31); spin<u64x8, 7>(c.ws, c.t<T_FKV>(), t2); }
  }
  STAMP(6);
  const bool zw = c.m == 2 + c.g;
  u32x4 pr[8], pz[2];
  u32 pq[8];
  float e8[8];
  if (zw) { ld128<400, 0>(pz[0], c.vl, c.rw, c.t<T_ZP>()); ld128<401, 1024>(pz[1], c.vl, c.rw, c.t<T_ZP>()); }
  sfor<0, 24>([&](auto I) { constexpr int i = decltype(I)::v;
    if constexpr (i < 16 && !(i & 1)) { ld128<402 + i / 2, 0>(pr[i / 2], vp, c.rw, c.t<T_KVP>() + i * 2048); SB(); }
    if constexpr (i == 18) { bar_w<8, 4>(); publish<3>(c.tag7, c.vpub, c.rw); SB(); }
    if constexpr (i == 19 || i == 21 || i == 23) { lda<5 + (i - 19) / 2>(c, a, v0); SB(); }
    phi4<i % 6, 4 * (i / 6)>(aQ, e8, pq + 2 * (i / 6)); });
#pragma unroll
  for (int i = 0; i < 8; ++i) tie(pq[i]);
  if (zw) {
    tie(pz[0]); tie(pz[1]);
    f32x4 u = __builtin_bit_cast(f32x4, pz[0]) + __builtin_bit_cast(f32x4, pz[1]);
    const auto r0 = __builtin_amdgcn_permlane32_swap(f2u(u[0]), f2u(u[2]), false, false);
    const auto r1 = __builtin_amdgcn_permlane32_swap(f2u(u[1]), f2u(u[3]), false, false);
    float w0 = u2f(r0[0]) + u2f(r0[1]), w1 = u2f(r1[0]) + u2f(r1[1]);
    w0 += u2f(__builtin_amdgcn_update_dpp(0u, f2u(w0), 0x128, 0xF, 0xF, false));
    w1 += u2f(__builtin_amdgcn_update_dpp(0u, f2u(w1), 0x128, 0xF, 0xF, false));
    const auto q0 = __builtin_amdgcn_permlane16_swap(f2u(w0), f2u(w0), false, false);
    const auto q1 = __builtin_amdgcn_permlane16_swap(f2u(w1), f2u(w1), false, false);
    *(LAS u32*)(s2 + 1024 + ((c.vl >> 1) & 56) + ((c.vl >> 7) & 4)) = pk2(u2f(q0[0]) + u2f(q0[1]), u2f(q1[0]) + u2f(q1[1]));
  }
  SB();
  waitv<4, 409>();
#pragma unroll
  for (int i = 0; i < 8; ++i) tie(pr[i]);
  STAMP(24);
  {
    f32x4 t = ((__builtin_bit_cast(f32x4, pr[0]) + __builtin_bit_cast(f32x4, pr[1])) + (__builtin_bit_cast(f32x4, pr[2]) + __builtin_bit_cast(f32x4, pr[3]))) +
              ((__builtin_bit_cast(f32x4, pr[4]) + __builtin_bit_cast(f32x4, pr[5])) + (__builtin_bit_cast(f32x4, pr[6]) + __builtin_bit_cast(f32x4, pr[7])));
    const auto r0 = __builtin_amdgcn_permlane32_swap(f2u(t[0]), f2u(t[2]), false, false);
    const auto r1 = __builtin_amdgcn_permlane32_swap(f2u(t[1]), f2u(t[3]), false, false);
    *(LAS u32*)aS = pk2(u2f(r0[0]) + u2f(r0[1]), u2f(r1[0]) + u2f(r1[1]));
  }
  bar_l();
  STAMP(25);
  u32x4 A0 = lds128(aA0), A1 = lds128(aA1);
  tie(A0); tie(A1);
  poll(fk, c.ws, c.t<T_SFL>());
  f32x16 Y0 = mm1<0>(A0, vec4(pq), f32x16{});
  f32x16 Y1 = mm1<0>(A1, vec4(pq + 4), f32x16{});
  const float i0 = __builtin_amdgcn_rcpf(Y0[8] + 1e-6f), i1 = __builtin_amdgcn_rcpf(Y1[0] + 1e-6f);
  u32x4 o0, o1;
#pragma unroll
  for (int i = 0; i < 4; ++i) { const f32x2 t = f32x2{Y0[2 * i], Y0[2 * i + 1]} * i0; o0[i] = pk2(t.x, t.y); }
#pragma unroll
  for (int i = 0; i < 4; ++i) { const f32x2 t = f32x2{Y1[8 + 2 * i], Y1[9 + 2 * i]} * i1; o1[i] = pk2(t.x, t.y); }
  SB();
  st128<410, 0>(o0, c.vl, c.rw, so + 20480);
  st128<411, 1024>(o1, c.vl, c.rw, so + 20480);
  SB();
  waitv<2, 307>();
#pragma unroll
  for (int i = 0; i < 8; ++i) tie(a[i]);
  polled(fk);
  mid = ready<2>(fk, c.tag7);
  return st0;
}

template <class F>
DI bool type_a(Ctx& c, Early& early, u32x4* a, bool& mid, F&& second STAMP_ARG) {
  typedef T1<1> P;
  u32x4 xf[32], fu[4];
  f32x16 aK, aV;
  u32 bias;
  lds_bases<1>(c, 2 * c.g);
  const u32 lane = c.vl >> 4, h = lane >> 5;
  u32 vT = (lane >> 2) * 3584 + (lane & 3) * 16;
  u32 at = L_R1 + c.g * 2048 + (8 * h + ((lane & 15) >> 2)) * 64 + (((lane >> 4) ^ c.g) & 1) * 32 + (lane & 3) * 8;
  u32 him = (lane & 16) ? 0xFFFFFFFFu : 0u, one = h ? 0u : 0x3F80u, row04 = (lane & 27) == 0 ? 0x3F803F80u : 0u;
  u32 sb = L_S1 + c.w * 2176 + c.vl, sz = L_S1 + c.w * 2176 + 2048 + ((lane & 19) + (lane & 4) * 2 + (lane & 8) / 2) * 4;
  u32 rb = L_S1 + c.g * 4 * 2176 + vsel(h, 2048 + (c.vl & 112), c.m * 512 + c.vl);
  u32 vk = vsel(h, vsel(lane < 40 && c.m == 2 + c.g, c.t<T_ZP>() + (c.vl & 112), OOB), c.t<T_KVP>() + c.vl), vf = lane < RK ? c.vl * 8 : OOB;
  tie(at); tie(him); tie(vT); tie(one); tie(row04); tie(sb); tie(sz); tie(rb); tie(vk); tie(vf);
  bar_w<3, 12>();
  STAMP(2);
  pass1<1>(c, xf, aK, aV, 0u, fu, second, [&](auto I) { if constexpr (decltype(I)::v == 2) bias = take_early(early); } STAMP_PASS);
  auto tfrag = [&](int s) { const u32 o = at + (s >> 1) * 4096 + (s & 1) * 1024; return ldstr2(o, o + 256); };
  STAMP(3);
  u32 pkk[8];
  float e8[8];
  f32x16 Z, aT;
  tile<32, 1, true>(xf, aV, fu,
    [&](auto I) { constexpr int s = decltype(I)::v;
      if constexpr (s == 9) bar_w<3 + NLATE, 107>();
      if constexpr (s == 29) { bar_w<8, 123>(); tie(early.pw); } },
    [&](auto I, u32x4& f) { constexpr int s = decltype(I)::v;
      if constexpr (s < 32) f = wstep<1, 0>(c, 0, s); else f = tfrag(s - 32); },
    [&](auto I) { constexpr int s = decltype(I)::v;
      if constexpr (s >= 1 && s <= 24) phi4<(s - 1) % 6, 4 * ((s - 1) / 6)>(aK, e8, pkk + 2 * ((s - 1) / 6));
      if constexpr (s == 25) Z = mm1<0>(u32x4{row04, row04, row04, row04}, vec4(pkk), f32x16{});
      if constexpr (s == 26) Z = mm1<0>(u32x4{row04, row04, row04, row04}, vec4(pkk + 4), Z);
      if constexpr (s == 28) aT = add_bias(bias, c.g ? 0u : one ^ 0x3F80u, f32x16{});
      if constexpr (s == 29) { tie(Z); *(LAS float*)sz = Z[0]; }
      SB();
      if constexpr (s == 0 || s == 3 || s == 7 || s == 10) { constexpr int i = s / 3;
        dma<120 + i, L_R1 + i * 8192, 0>(c.wl, vT, c.rW2, c.t<T_ST>() + ((c.t<T_ROT>() + 2 * i + c.g) & 7) * (64 * 3584)); }
      if constexpr (s >= 13 && s <= 27 && (s & 1)) P::dmaW3<(s - 13) / 2>(c);
      if constexpr (s == 29 || s == 31) dmaX0<3 + (s - 29) / 2>(c); });
  STAMP(4);
  f32x16 R, aU;
  u32 v[8];
  f32x4 t4[4];
  tile<16, 0, false>(xf, aT, fu,
    [&](auto I) { constexpr int s = decltype(I)::v;
      if constexpr (s == 5) bar_v<6, 115>();
      if constexpr (s == 11) bar();
      if constexpr (s == 15) { waitv<3, 420>(); st32<9>((u32)c.tag7, vf, c.rw, c.t<T_FKV>()); } },
    [&](auto I, u32x4& f) { constexpr int s = decltype(I)::v; if constexpr (s < 16) f = tfrag(s); else f = wstep<1, 2>(c, 0, s - 16); },
    [&](auto I) { constexpr int s = decltype(I)::v;
      if constexpr (s == 1) {
        asm volatile("s_nop 1" : "+v"(aV));
#pragma unroll
        for (int i = 0; i < 8; ++i) { v[i] = pk2(aV[2 * i], aV[2 * i + 1]); tie(v[i]); }
        SB();
        R = mm1<0>(vec4(pkk), vec4(v), f32x16{});
      }
      if constexpr (s == 2) R = mm1<0>(vec4(pkk + 4), vec4(v + 4), R);
      if constexpr (s == 4) {
        asm volatile("s_nop 1" : "+v"(R));
#pragma unroll
        for (int k = 0; k < 8; k += 4)
          *(LAS f32x4*)(sb + k * 256) = f32x4{bfi(him, R[8 + k], R[k]), bfi(him, R[9 + k], R[1 + k]), bfi(him, R[10 + k], R[2 + k]), bfi(him, R[11 + k], R[3 + k])};
      }
      if constexpr (s == 5) {
#pragma unroll
        for (int i = 0; i < 4; ++i) t4[i] = *(LAS f32x4*)(rb + i * 2176);
      }
      if constexpr (s == 6) {
        u32x4 o;
#pragma unroll
        for (int i = 0; i < 4; ++i) { u32 u = f2u((t4[0][i] + t4[1][i]) + (t4[2][i] + t4[3][i])); tie0(u); o[i] = u; }
        st128<420, 0>(o, vk, c.rw, 0);
      }
      if constexpr (s == 12) aU = add_bias(bias, one, f32x16{});
      SB();
      if constexpr (s == 0 || s == 2) dmaX0<5 + s / 2>(c);
      if constexpr (s == 9) dmaX0<7>(c);
      if constexpr (s == 11 || s == 13 || s == 15) dmaX0<(s - 11) / 2>(c); });
  STAMP(24);
  const u32 sc = L_R1 + c.w * 2048 + c.vl, sr = sc ^ 8192;
  const u32 so = c.t<T_SO>() + 28672;
  float hT[8], e[4];
  f32x4 p[2];
  u32 pk[4];
  bool st0;
  u32 vs;
  u64x4 f4;
  tile<32, 0, false>(xf, aU, fu,
    [&](auto I) { constexpr int s = decltype(I)::v;
      if constexpr (s == 2) bar_l();
      if constexpr (s == 7 || s == 25) bar();
      if constexpr (s == 27) { waitv<5, 1>(); publish<3>(c.tag7, c.vpub, c.rw); } },
    [&](auto I, u32x4& f) { constexpr int s = decltype(I)::v; if constexpr (s < 32) f = wstep<1, 2>(c, 0, s); },
    [&](auto I) { constexpr int s = decltype(I)::v;
      if constexpr (s == 1) { *(LAS f32x4*)sc = f32x4{aT[8], aT[9], aT[10], aT[11]}; *(LAS f32x4*)(sc + 1024) = f32x4{aT[12], aT[13], aT[14], aT[15]}; }
      if constexpr (s == 2) { p[0] = *(LAS f32x4*)sr; p[1] = *(LAS f32x4*)(sr + 1024); }
      if constexpr (s == 3 || s == 4) {
#pragma unroll
        for (int i = 0; i < 4; ++i) { hT[4 * (s - 3) + i] = aT[4 * (s - 3) + i] + p[s - 3][i]; tie(hT[4 * (s - 3) + i]); }
      }
      SB();
      if constexpr (s >= 5 && s < 17) mish4<(s - 5) % 6, 6, 4 * ((s - 5) / 6)>(hT, e, pk + 2 * ((s - 5) / 6));
      if constexpr (s == AP0) poll(f4, c.ws, c.t<T_SFL>());
      if constexpr (s == 3 || s == 5) dmaX2<2 + (s - 3) / 2>(c);
      if constexpr (s == 7 || s == 9) dmaX2<(s - 7) / 2>(c);
      if constexpr (s == 11) dmaX1<0>(c);
      if constexpr (s == 13) { polled(f4); st0 = ready<1>(f4, c.tag7); vs = st0 ? c.vl : OOB; lda<0>(c, a, vs); }
      if constexpr (s >= 14 && s <= 26 && !(s & 1)) lda<1 + (s - 14) / 2>(c, a, vs);
      if constexpr (s == 17) st128<1, 0>(vec4(pk), c.vl, c.rw, c.t<T_AUX>());
      if constexpr (s == 28 || s == 30) dmaX1<1 + (s - 28) / 2>(c); });
  STAMP(25);
  dmaX1<3>(c); dmaX1<4>(c); SB();
  u32x4 y = mish8(aU, 0); SB();
  dmaX1<5>(c);
  st128<2, 0>(y, c.vl, c.rw, so); SB();
  u64x8 fl;
  poll(fl, c.ws, c.t<T_SFL>()); SB();
  y = mish8(aU, 8); SB();
  st128<3, 1024>(y, c.vl, c.rw, so); SB();
  bar_w<2, 215>();
  polled(fl);
  mid = ready<2>(fl, c.tag7);
#pragma unroll
  for (int i = 0; i < 8; ++i) tie(a[i]);
  return st0;
}

DI constexpr u32 bblock(int s) {
  if (s < 16) return ((s >> 2) & 1) * 16384 + (s >> 3) * 8192 + (s & 3) * 2048;
  if (s < 20) return 8192 + (s - 16) * 2048;
  if (s < 28) return (s - 20) * 2048;
  return (s < 32 ? 0 : 16384) + (s & 3) * 2048;
}

struct P2 {
  u32 a0;
  u32 ab[3][2];
  u32x4 f0[3], f1[3];
  f32x16 cB0, cB1, cA0, cA1;
  DI u32x4 rd(int s, int n) const {
    const int sec = s < 16 ? 0 : (s >= 20 && s < 28) ? 1 : 2;
    return ldstr2(ab[sec][n] + bblock(s), ab[sec][n] + bblock(s) + 1024);
  }
  template <int B, int E, class F, class G> DI void run(u32x4 (&a)[36], F&& pre, G&& post) {
    sfor<B, E>([&](auto I) {
      constexpr int S = decltype(I)::v;
      constexpr bool PA = S >= 20 && S < 28, Z = S == 0 || S == 20;
      pre(I); SB();
      if constexpr (PA) cA0 = mm1<0>(f0[S % 3], a[S], Z ? f32x16{} : cA0); else cB0 = mm1<0>(f0[S % 3], a[S], Z ? f32x16{} : cB0);
      SB();
      if constexpr (S + 2 < 36) { f0[(S + 2) % 3] = rd(S + 2, 0); SB(); }
      post(I, IC<0>{}); SB();
      if constexpr (PA) cA1 = mm1<0>(f1[S % 3], a[S], Z ? f32x16{} : cA1); else cB1 = mm1<0>(f1[S % 3], a[S], Z ? f32x16{} : cB1);
      SB();
      if constexpr (S + 2 < 36) { f1[(S + 2) % 3] = rd(S + 2, 1); SB(); }
      post(I, IC<1>{}); SB();
    });
  }
};

}

__global__ __launch_bounds__(512, 2) void gated_mix_kernel(Args) {
  __shared__ __attribute__((aligned(1024))) char smem[L_END];
  STAMP(0);
  const u32 b = blockIdx.x, type = b >> 7;
  Ctx c;
  c.w = __builtin_amdgcn_readfirstlane(threadIdx.x >> 6);
  const void* ka = (const void*)__builtin_amdgcn_kernarg_segment_ptr();
  u32x16 k0;
  u32x8 kw, kb;
  asm volatile("s_load_dwordx16 %0, %5, %6 ; sop\n\ts_load_dwordx16 %2, %8, 0x0 ; sop\n\ts_load_dwordx8 %3, %8, %9 ; sop\n\t"
               "s_load_dwordx8 %4, %8, 0x70 ; sop\n\ts_load_dwordx16 %1, %5, %7 ; sop"
               : "=&s"(c.t0), "=&s"(c.t1), "=&s"(k0), "=&s"(kw), "=&s"(kb)
               : "s"(&tab), "s"((b * 8 + c.w) * 128), "s"((b * 8 + c.w) * 128 + 64), "s"(ka), "s"(64 + type * 16) : "memory");
  SB();
  Early early;
  {
    u64 pc; asm volatile("s_getpc_b64 %0" : "=s"(pc));
    const u32 ln = c.w * 32 + (b >> 3);
    asm volatile("global_load_dword %0, %1, %2" : "=v"(early.pf) : "v"((ln < CODE_LINES ? ln : 0) * 128), "s"(pc & ~127ull) : "memory");
  }
  const u32 lane = threadIdx.x & 63;
  c.m = c.w & 3; c.g = c.w >> 2; c.jj = (b >> 6) & 1;
  const u32 lbase = (u32)(uintptr_t)(LAS char*)smem;
  c.wl = lbase + c.w * 1024;
  const u32 p1 = 3584 - 512 * type, p2 = 3072 + 512 * type;
  const u32 lrow = lane >> 3, lpos = lane & 7;
  c.vx = lrow * 1024 + ((lpos ^ ((4 * (c.w & 1) + (lane >> 4)) & 7)) * 16);
  const u32 wsw = (lpos ^ (((lane >> 4) & 1) << 2)) * 16;
  c.vW1 = lrow * p1 + wsw; c.vW2 = lrow * p2 + wsw; c.vB = lrow * 1024 + wsw;
  c.vl = lane * 16;
  u32 wg = c.wl & 3072, vg = c.vl + (c.wl & 2048);
  asm volatile("" : "+s"(wg), "+s"(c.wl));
  tie(c.vx); tie(c.vW1); tie(c.vW2); tie(c.vB); tie(c.vl); tie(vg);
  SB();
  asm volatile("s_waitcnt lgkmcnt(0)" : "+s"(c.t0), "+s"(c.t1), "+s"(k0), "+s"(kw), "+s"(kb) :: "memory");
  STAMP(30);
  c.rx = __builtin_shufflevector(k0, k0, 0, 1, 2, 3); c.rw = __builtin_shufflevector(k0, k0, 4, 5, 6, 7);
  c.rw2 = __builtin_shufflevector(k0, k0, 8, 9, 10, 11); c.rwo = __builtin_shufflevector(k0, k0, 12, 13, 14, 15);
  c.rW1 = __builtin_shufflevector(kw, kw, 0, 1, 2, 3); c.rW2 = __builtin_shufflevector(kw, kw, 4, 5, 6, 7);
  c.ws = ((u64)k0[5] << 32) | k0[4];
  dma<10, ring(0), 0>(c.wl, c.vx, c.rx, c.t<T_SX0>());
  dma<11, ring(0) + 8192, 65536>(c.wl, c.vx, c.rx, c.t<T_SX0>());
  dma<12, ring(0) + 16384, 0>(c.wl, c.vW1, c.rW1, c.t<T_SW0>());
  u64 ep, pe, pl;
  asm volatile("s_load_dwordx2 %0, %3, 0x0 ; sop\n\ts_load_dwordx2 %1, %4, %5 ; sop\n\ts_load_dwordx2 %2, %4, %6 ; sop"
               : "=&s"(ep), "=&s"(pe), "=&s"(pl) : "s"(c.ws), "s"(ka), "s"(c.t<T_E2>() >> 24), "s"(c.t<T_L2>() >> 24) : "memory");
  if (type == 0 && c.g == 0) dma<13, L_GW, 0>(wg, vg, __builtin_shufflevector(kb, kb, 4, 5, 6, 7), 0u);
  bar();
  dma<20, ring(1), 0>(c.wl, c.vx, c.rx, c.t<T_SX1>());
  dma<21, ring(1) + 8192, 65536>(c.wl, c.vx, c.rx, c.t<T_SX1>());
  dma<22, ring(1) + 16384, 0>(c.wl, c.vW1, c.rW1, c.t<T_SW1>());
  SB();
  STAMP(1);
  P2 p;
  {
    c.vpub = lane < RP ? c.t<T_PUB>() + lane * 128 : OOB;
    const u32 h = lane >> 5, q = (lane & 15) >> 2, pp = lane & 3, blk = (lane >> 4) & 1, kq = c.m >> 1;
    p.a0 = lbase + (4 * h + q) * 128 + (((4 * kq + 2 * blk + (pp >> 1)) ^ ((q >> 1) << 2)) * 16) + 8 * (pp & 1);
    tie(c.vpub); tie(p.a0);
  }
  auto reqoff = [&](u32 off, u32 delta, u32 pk) { return off + vsel(lane < ((pk >> 16) & 255), (lane & 31) * (pk & 0xFFFF) + (lane >> 5) * delta, 0); };
  u32 ve = reqoff(c.t<T_E0>(), c.t<T_E1>(), c.t<T_E2>()), vlate = reqoff(c.t<T_L0>(), c.t<T_L1>(), c.t<T_L2>());
  u32 vb = (lane & 31) * 2 + vsel(lane < 32, c.t<T_BIAS>() & 0xFFFF, c.t<T_BIAS>() >> 16);
  tie(ve); tie(vlate); tie(vb);
  early.pw = 0;
  auto second = [&](bool late) {
    if (!late) {
      dma<30, ring(2), 0>(c.wl, c.vx, c.rx, c.t<T_SX2>());
      dma<31, ring(2) + 8192, 65536>(c.wl, c.vx, c.rx, c.t<T_SX2>());
      dma<32, ring(2) + 16384, 0>(c.wl, c.vW1, c.rW1, c.t<T_SW2>());
      asm volatile("buffer_load_ushort %0, %1, %2, 0 offen ; op 15" : "=v"(early.bias) : "v"(vb), "s"(__builtin_shufflevector(kb, kb, 0, 1, 2, 3)) : "memory");
    }
    asm volatile("global_load_dword %0, %1, %2 ; op 17" : "+v"(early.pw) : "v"(late ? vlate : ve), "s"(late ? pl : pe) : "memory");
    SB();
  };
  asm volatile("s_waitcnt lgkmcnt(0)" : "+s"(ep), "+s"(pe), "+s"(pl) :: "memory");
  c.tag7 = (ep + TAG_STEP) | 7;
  const u64 ws = c.ws;

  u32x4 a[36];
  bool st0, mid;
  if (type == 0) st0 = type_b(c, early, a, mid, second STAMP_PASS);
  else st0 = type_a(c, early, a, mid, second STAMP_PASS);
  u64x8 fl;
  STAMP(7);

  const u32 sg = c.t<T_SG>(), sp0 = c.t<T_SP0>(), sp1 = c.t<T_SP1>();
  {
    const u32 kq = c.m >> 1;
    p.ab[0][0] = p.a0 + L_R0 + kq * 32768; p.ab[1][0] = p.a0 + L_R1 + kq * 16384; p.ab[2][0] = p.a0 + L_R2 + kq * 24576;
#pragma unroll
    for (int i = 0; i < 3; ++i) { p.ab[i][1] = p.ab[i][0] ^ 64; tie(p.ab[i][0]); tie(p.ab[i][1]); }
  }
  p.f0[0] = p.rd(0, 0); p.f1[0] = p.rd(0, 1); p.f0[1] = p.rd(1, 0); p.f1[1] = p.rd(1, 1); SB();
  if (__builtin_expect(!st0, 0)) {
    STAMP(27);
    spin<u64x4, 1>(ws, c.t<T_SFL>(), c.tag7);
#pragma unroll
    for (int i = 0; i < 2; ++i) { rld128<0>(a[4 * i], c.vl, c.rw, c.t<T_SIN>() + i * 4096); rld128<1024>(a[4 * i + 1], c.vl, c.rw, c.t<T_SIN>() + i * 4096);
                                  rld128<2048>(a[4 * i + 2], c.vl, c.rw, c.t<T_SIN>() + i * 4096); rld128<3072>(a[4 * i + 3], c.vl, c.rw, c.t<T_SIN>() + i * 4096); }
    drain();
#pragma unroll
    for (int i = 0; i < 8; ++i) tie(a[i]);
    STAMP(29);
  }
  STAMP(8);
  u32 gl0, gl1;
  float gate;
  auto none = [](auto) {};
  auto none2 = [](auto, auto) {};
  auto mk_gate = [&]() {
    tie(gl0); tie(gl1);
    float l = u2f(gl0) + u2f(gl1);
    const auto s = __builtin_amdgcn_permlane32_swap(f2u(l), f2u(l), false, false);
    gate = __builtin_amdgcn_rcpf(1.f + __builtin_amdgcn_exp2f(-1.44269504f * (u2f(s[0]) + u2f(s[1]))));
    tie(gate);
  };
  auto ld_gate = [&]() { ld32<340, 0>(gl0, c.vl >> 2, c.rw, sg); ld32<341, 256>(gl1, c.vl >> 2, c.rw, sg); };
  auto scale_a = [&](auto I, auto J) { constexpr int S = decltype(I)::v, H = decltype(J)::v;
    if constexpr (S >= 30 && S < 34) {
#pragma unroll
      for (int i = 4 * (S - 30); i < 4 * (S - 30) + 4; ++i) { float u = (H ? p.cA1[i] : p.cA0[i]) * gate; tie(u); if (H) p.cA1[i] = u; else p.cA0[i] = u; }
    } };
  float r0[16], r1[16];
  auto mix = [&]() {
    const float ng = 1.f - gate;
#pragma unroll
    for (int i = 0; i < 16; ++i) { r0[i] = __builtin_fmaf(ng, p.cB0[i], p.cA0[i]); tie(r0[i]); }
#pragma unroll
    for (int i = 0; i < 16; ++i) { r1[i] = __builtin_fmaf(ng, p.cB1[i], p.cA1[i]); tie(r1[i]); }
  };
  if (c.g) {
    __builtin_amdgcn_s_setprio(1);
    if (!mid) spin<u64x8, 2>(ws, c.t<T_SFL>(), c.tag7);
    STAMP(28);
    ld_gate();
    p.run<0, 8>(a, none, [&](auto I, auto J) { constexpr int S = decltype(I)::v, H = decltype(J)::v;
                  if constexpr (S < 4 || H == 0) lda<S < 4 ? 8 + 2 * S + H : 12 + S>(c, a, c.vl);
                  if constexpr (S == 2 && H == 1) { asm volatile("s_waitcnt vmcnt(8)" ::: "memory"); publish<7>(c.tag7, c.vpub, c.rw); } });
    STAMP(9);
    p.run<8, 20>(a, [&](auto I) { constexpr int S = decltype(I)::v; waitv<19 - S + (S < 14), 300 + S>(a[S]); },
                 [&](auto I, auto J) { constexpr int S = decltype(I)::v, H = decltype(J)::v;
                   if constexpr (S == 9 && H == 0) mk_gate();
                   if constexpr (S == 13 && H == 0) poll(fl, ws, c.t<T_SFL>()); });
    polled(fl);
    STAMP(10);
    if (!ready<4>(fl, c.tag7)) spin<u64x8, 4>(ws, c.t<T_SFL>(), c.tag7);
    STAMP(11);
    sfor<20, 30>([&](auto I) { lda<decltype(I)::v>(c, a, c.vl); });
    STAMP(12);
    p.run<20, 36>(a, [&](auto I) { constexpr int S = decltype(I)::v; waitv<(S < 23 ? 29 + 2 * (S - 20) : 35) - S, 300 + S>(a[S]); },
                 [&](auto I, auto J) { constexpr int S = decltype(I)::v, H = decltype(J)::v;
                   if constexpr (S < 23) lda<30 + 2 * (S - 20) + H>(c, a, c.vl);
                   scale_a(I, J); });
    mix();
    STAMP(13);
    u32 y[16];
#pragma unroll
    for (int i = 0; i < 8; ++i) { y[i] = pk2(r0[2 * i], r0[2 * i + 1]); y[8 + i] = pk2(r1[2 * i], r1[2 * i + 1]); }
    if constexpr (TAGX) {
      const u32 tg = (u32)c.tag7;
      st128<350, 0>(u32x4{y[0], y[1], y[2], tg}, c.vl, c.rw, sp0); st128<351, 1024>(u32x4{y[3], y[4], y[5], tg}, c.vl, c.rw, sp0);
      st128<352, 2048>(u32x4{y[6], y[7], tg, tg}, c.vl, c.rw, sp0);
      st128<353, 0>(u32x4{y[8], y[9], y[10], tg}, c.vl, c.rw, sp1); st128<354, 1024>(u32x4{y[11], y[12], y[13], tg}, c.vl, c.rw, sp1);
      st128<355, 2048>(u32x4{y[14], y[15], tg, tg}, c.vl, c.rw, sp1);
      STAMP(14);
      bar();
    } else {
      st128<350, 0>(vec4(y), c.vl, c.rw, sp0); st128<351, 1024>(vec4(y + 4), c.vl, c.rw, sp0);
      st128<352, 0>(vec4(y + 8), c.vl, c.rw, sp1); st128<353, 1024>(vec4(y + 12), c.vl, c.rw, sp1);
      const u32 vp = c.vl == 0 ? WS_FP + ((b ^ 1) * 2 + (c.m & 1)) * 128 + (c.m >> 1) * 8 : OOB;
      drain();
      publish<1>(c.tag7, vp, c.rw);
      STAMP(14);
    }
    if (b == 0 && c.w == 4) {
      const u32 ln = c.vl >> 4, ve = WS_FPROG + ((ln >> 4) * 16 + ((ln & 7) >> 1)) * RP * 128 + ((ln >> 3) & 1) * 32 + (ln & 1) * 16;
      for (u32 n = 0; n < SPIN_CAP; ++n) {
        u32x2 fe[4];
#pragma unroll
        for (int i = 0; i < 4; ++i) ld64<8>(fe[i], ve + i * 8192 * RP, c.rw);
        drain();
        bool ok = true;
#pragma unroll
        for (int i = 0; i < 4; ++i) { tie(fe[i]); ok &= ready<2>(((u64)fe[i].y << 32) | fe[i].x, c.tag7); }
        if (__all(ok)) break;
      }
      publish<0>(c.tag7, c.vl == 0 ? WS_E : OOB, c.rw);
    }
  } else {
    p.run<0, 8>(a, none, [&](auto I, auto J) { constexpr int S = decltype(I)::v, H = decltype(J)::v;
                  if constexpr (S == 1 && H == 0) poll(fl, ws, c.t<T_SFL>());
                  if constexpr (S == 4 && H == 1) { drain(); publish<7>(c.tag7, c.vpub, c.rw); } });
    polled(fl);
    if (__builtin_expect(!ready<2>(fl, c.tag7), 0)) { STAMP(28); spin<u64x8, 2>(ws, c.t<T_SFL>(), c.tag7); }
    STAMP(9);
    ld_gate();
    sfor<8, 20>([&](auto I) { lda<decltype(I)::v>(c, a, c.vl); });
    p.run<8, OCHK>(a, [&](auto I) { constexpr int S = decltype(I)::v; waitv<19 - S, 300 + S>(a[S]); },
                 [&](auto I, auto J) { constexpr int S = decltype(I)::v, H = decltype(J)::v;
                   if constexpr (S == OCHK - 6 && H == 0) poll(fl, ws, c.t<T_SFL>());
                   if constexpr (S == 9 && H == 0) mk_gate(); });
    polled(fl);
    STAMP(10);
    if (!ready<4>(fl, c.tag7)) { spin<u64x8, 4>(ws, c.t<T_SFL>(), c.tag7); __builtin_amdgcn_s_sleep(OSLP); }
    STAMP(11);
    u64x2 fp;
    u32x4 ro;
    const u32 sfp = WS_FP + (b * 2 + (c.m & 1)) * 128;
    constexpr int N0 = 2 * (OCHK - 14);
    sfor<20, 20 + N0>([&](auto I) { lda<decltype(I)::v>(c, a, c.vl); });
    p.run<OCHK, 36>(a, [&](auto I) { constexpr int S = decltype(I)::v;
                   if constexpr (S < 20) waitv<19 - S + 2 * (S - 14), 300 + S>(a[S]);
                   else waitv<(S < 22 ? 31 + 2 * (S - 20) : 35) - S, 300 + S>(a[S]); },
                 [&](auto I, auto J) { constexpr int S = decltype(I)::v, H = decltype(J)::v;
                   if constexpr (S < 22) lda<20 + 2 * (S - 14) + H>(c, a, c.vl);
                   if constexpr (!TAGX && S == 28 && H == 0) poll(fp, ws, sfp);
                   scale_a(I, J); });
    u32x4 pp[4];
    u32x3 pt[2];
    bool pok = true;
    if constexpr (!TAGX) { polled(fp); pok = ready<1>(fp, c.tag7); }
    { const u32 v = pok ? c.vl : OOB;
      ld128<360, 0>(pp[0], v, c.rw, sp0); ld128<361, 1024>(pp[1], v, c.rw, sp0); ld128<362, 0>(pp[2], v, c.rw, sp1); ld128<363, 1024>(pp[3], v, c.rw, sp1);
      if constexpr (TAGX) { ld96<364, 2048>(pt[0], v, c.rw, sp0); ld96<365, 2048>(pt[1], v, c.rw, sp1); } }
    asm volatile("s_load_dwordx4 %0, %1, 0x90 ; sop" : "=s"(ro) : "s"(ka) : "memory");
    mix();
    STAMP(13);
    const u32 sc = lbase + L_R3 + c.m * 4096 + c.vl, sr = sc ^ 8192;
#pragma unroll
    for (int i = 0; i < 4; ++i) *(LAS f32x4*)(sc + i * 1024) = f32x4{r1[4 * i], r1[4 * i + 1], r1[4 * i + 2], r1[4 * i + 3]};
    polled(ro);
    auto reload = [&]() {
      rld128<0>(pp[0], c.vl, c.rw, sp0); rld128<1024>(pp[1], c.vl, c.rw, sp0); rld128<0>(pp[2], c.vl, c.rw, sp1); rld128<1024>(pp[3], c.vl, c.rw, sp1);
      if constexpr (TAGX) { rld96<2048>(pt[0], c.vl, c.rw, sp0); rld96<2048>(pt[1], c.vl, c.rw, sp1); } };
    auto landed = [&]() { drain(); tie(pp[0]); tie(pp[1]); tie(pp[2]); tie(pp[3]); if constexpr (TAGX) { tie(pt[0]); tie(pt[1]); } };
    if (!pok) {
      spin<u64x2, 1>(ws, sfp, c.tag7);
      STAMP(12);
      reload();
    }
    bar_l();
#pragma unroll
    for (int i = 0; i < 4; ++i) {
      const f32x4 v = *(LAS f32x4*)(sr + i * 1024);
#pragma unroll
      for (int k = 0; k < 4; ++k) { r0[4 * i + k] += v[k]; tie(r0[4 * i + k]); }
    }
    STAMP(26);
    landed();
    if constexpr (TAGX) {
      const u32 tg = (u32)c.tag7;
      for (u32 n = 0; n < SPIN_CAP; ++n) {
        const u32 bad = ((pp[0].w ^ tg) | (pp[1].w ^ tg) | (pt[0].z ^ tg)) | ((pp[2].w ^ tg) | (pp[3].w ^ tg) | (pt[1].z ^ tg));
        if ((EXP & 16) || __all(bad == 0)) break;
        reload();
        landed();
      }
    }
    STAMP(14);
#pragma unroll
    for (int i = 0; i < 8; ++i) {
      const u32 u0 = TAGX ? (i < 6 ? pp[i / 3][i % 3] : pt[0][i - 6]) : pp[i >> 2][i & 3];
      const u32 u1 = TAGX ? (i < 6 ? pp[2 + i / 3][i % 3] : pt[1][i - 6]) : pp[2 + (i >> 2)][i & 3];
      r0[2 * i] += u2f(u0 << 16) + u2f(u1 << 16); r0[2 * i + 1] += u2f(u0 & 0xFFFF0000u) + u2f(u1 & 0xFFFF0000u);
    }
    const u32 vo = ((c.vl << 6) & 0x7C00) + ((c.vl >> 5) & 16);
    u32x4 y[2];
#pragma unroll
    for (int i = 0; i < 2; ++i) {
      u32x4 t = {pk2(r0[8 * i], r0[8 * i + 1]), pk2(r0[8 * i + 2], r0[8 * i + 3]), pk2(r0[8 * i + 4], r0[8 * i + 5]), pk2(r0[8 * i + 6], r0[8 * i + 7])};
      nop2(t);
      const auto s0 = __builtin_amdgcn_permlane32_swap(t[0], t[2], false, false);
      const auto s1 = __builtin_amdgcn_permlane32_swap(t[1], t[3], false, false);
      y[i] = u32x4{s0[0], s1[0], s0[1], s1[1]};
    }
    asm volatile("buffer_store_dwordx4 %0, %2, %3, %4 offen\n\tbuffer_store_dwordx4 %1, %2, %3, %4 offen offset:32\n\ts_nop 1"
                 ::"v"(y[0]), "v"(y[1]), "v"(vo), "s"(ro), "s"(c.t<T_SY>()) : "memory");
  }
}

extern "C" void kernel_launch(void* const* d_in, const int* in_sizes, int n_in, void* d_out, int out_size, void* d_ws, size_t ws_size,
                              hipStream_t stream) {
  constexpr u32 F = 0x00020000;
  Args a;
  a.x = {d_in[0], (EXP & 64) ? 0u : 2048u * 1024, F}; a.ws = {d_ws, (EXP & 32) ? 0u : WS_END, F};
  a.w2 = {d_in[6], (EXP & 1) ? 0u : 1792u * 1024, F}; a.wo = {d_in[2], (EXP & 1) ? 0u : 512u * 1024, F};
  a.w1a = {d_in[4], (EXP & 8) ? 0u : 512u * 3584, F}; a.wqkv = {d_in[1], (EXP & 8) ? 0u : 512u * 3072, F}; a.w1b = a.w1a;
  a.b1 = {d_in[5], 3584, F}; a.gw = {d_in[7], 1024, F}; a.out = {d_out, 2048u * 1024, F};
  gated_mix_kernel<<<256, 512, 0, stream>>>(a);
}
```
